```python
import torch

def _softmax(x, axis=-1):
    return torch.softmax(x, dim=axis)
def _gelu(x):
    return torch.nn.functional.gelu(x, approximate="tanh")
def _silu(x):
    return torch.nn.functional.silu(x)
def _sigmoid(x):
    return torch.sigmoid(x)
def _layernorm(x, axis=-1, eps=1e-5):
    m = x.mean(dim=axis, keepdim=True)
    v = x.var(dim=axis, keepdim=True, unbiased=False)
    return (x - m) / torch.sqrt(v + eps)
def _scatter_add(tgt, idx, src, axis=0):
    return tgt.clone().index_add_(axis, idx, src)
def _segment_sum(src, seg, n):
    out = torch.zeros((n,) + src.shape[1:], dtype=src.dtype)
    return out.index_add_(0, seg, src)
def _pad(x, pw):
    flat = tuple(v for pair in reversed(pw) for v in pair)
    return torch.nn.functional.pad(x, flat)

kernel_name = 'sequential_preLN__ssm_conv_s4d__mixer_s_mlp__prompt'

def setup_inputs():

    _w = (lambda a: (lambda t: t.to(torch.bfloat16) if t.is_floating_point() else t)(torch.as_tensor(a)))
    def _A_setup():
        Bsz, L, D, N = 1, 2048, 512, 128
        return {
            "u": _w((lambda *s: torch.randn(*s, dtype=torch.bfloat16) * 0.05)(Bsz, L, D)),
            "log_A_real": _w((lambda *s: torch.randn(*s, dtype=torch.bfloat16) * 6.4)(D, N) * 0.5),
            "A_imag": _w((lambda *s: torch.randn(*s, dtype=torch.bfloat16) * 0.05)(D, N) * 3.14),
            "C_real": _w((lambda *s: torch.randn(*s, dtype=torch.bfloat16) * 0.05)(D, N) / N ** 0.5),
            "C_imag": _w((lambda *s: torch.randn(*s, dtype=torch.bfloat16) * 0.05)(D, N) / N ** 0.5),
            "log_dt": _w((lambda *s: torch.randn(*s, dtype=torch.bfloat16) * 0.05)(D) * 0.5 - 4.0),
            "D_skip": _w((lambda *s: torch.randn(*s, dtype=torch.bfloat16) * 0.05)(D)),
        }
    _A_in = _A_setup()

    def _B_setup():
        B, S, D, F = 1, 2048, 512, 1792
        return {
            "x":  _w((lambda *s: torch.randn(*s, dtype=torch.bfloat16) * 0.05)(B, S, D)),
            "w1": _w((lambda *s: torch.randn(*s, dtype=torch.bfloat16) * 6.4)(D, D) * (D ** -0.5)),
            "w2": _w((lambda *s: torch.randn(*s, dtype=torch.bfloat16) * 6.4)(D, D) * (D ** -0.5)),
            "w3": _w((lambda *s: torch.randn(*s, dtype=torch.bfloat16) * 6.4)(D, F) * (D ** -0.5)),
            "w4": _w((lambda *s: torch.randn(*s, dtype=torch.bfloat16) * 6.4)(F, D) * (F ** -0.5)),
        }
    _B_in = _B_setup()

    return {
        "A_u": _A_in["u"],
        "A_log_A_real": _A_in["log_A_real"],
        "A_A_imag": _A_in["A_imag"],
        "A_C_real": _A_in["C_real"],
        "A_C_imag": _A_in["C_imag"],
        "A_log_dt": _A_in["log_dt"],
        "A_D_skip": _A_in["D_skip"],
        "B_x": _B_in["x"],
        "B_w1": _B_in["w1"],
        "B_w2": _B_in["w2"],
        "B_w3": _B_in["w3"],
        "B_w4": _B_in["w4"],
    }

def reference(A_u, A_log_A_real, A_A_imag, A_C_real, A_C_imag, A_log_dt, A_D_skip, B_x, B_w1, B_w2, B_w3, B_w4):
    def _A_ref(u, log_A_real, A_imag, C_real, C_imag, log_dt, D_skip):
        L = u.shape[1]
        dt = torch.exp(log_dt)
        A = -torch.exp(log_A_real) + 1j * A_imag
        C = (C_real + 1j * C_imag) * dt[:, None]
        pos = (lambda x, dt: x.to(dt))(torch.arange(L), log_dt.dtype)
        P = torch.exp((A * dt[:, None])[:, :, None] * pos[None, None, :])
        K = 2.0 * torch.einsum("dn,dnl->dl", C, P).real
        L2 = 2 * L
        u_t = (lambda x, *d: x.permute(*d))(u, 0, 2, 1)
        Kf = (lambda x, *a, **k: torch.fft.rfft(x.float(), *a, **k))(K, L2)
        Uf = (lambda x, *a, **k: torch.fft.rfft(x.float(), *a, **k))(u_t, L2)
        y = (lambda x, *a, **k: torch.fft.irfft(x, *a, **k).to(torch.bfloat16))(Uf * Kf[None, :, :], L2)[:, :, :L]
        y = (lambda x, *d: x.permute(*d))(y, 0, 2, 1)
        return y + u * D_skip[None, None, :]

    def _B_ref(x, w1, w2, w3, w4):
        B, S, D = x.shape
        h = _gelu(_layernorm(x, axis=-1) @ w1)
        c = D // 4
        g0 = _pad(h[:, :, 0*c:1*c], ((0, 0), (1, 0), (0, 0)))[:, :S, :]
        g1 = _pad(h[:, :, 1*c:2*c], ((0, 0), (0, 1), (0, 0)))[:, 1:, :]
        g2 = _pad(h[:, :, 2*c:3*c], ((0, 0), (2, 0), (0, 0)))[:, :S, :]
        g3 = _pad(h[:, :, 3*c:   ], ((0, 0), (0, 2), (0, 0)))[:, 2:, :]
        hs = torch.concatenate([g0, g1, g2, g3], axis=-1) @ w2
        x1 = x + hs
        h2 = _gelu(_layernorm(x1, axis=-1) @ w3) @ w4
        return x1 + h2

    x = A_u
    h = x + _A_ref(_layernorm(x, axis=-1), A_log_A_real, A_A_imag, A_C_real, A_C_imag, A_log_dt, A_D_skip)
    h2 = h + _B_ref(_layernorm(h, axis=-1), B_w1, B_w2, B_w3, B_w4)
    return h2
```

```cpp
#include <hip/hip_runtime.h>
#include <hip/hip_bf16.h>
#include <stdint.h>

#define DI __device__ __forceinline__
typedef __attribute__((ext_vector_type(8))) short bf16x8;
typedef __attribute__((ext_vector_type(8))) _Float16 f16x8;
typedef __attribute__((ext_vector_type(2))) _Float16 f16x2;
typedef __attribute__((ext_vector_type(2))) __bf16 bf16x2;
typedef __attribute__((ext_vector_type(16))) float f32x16;
typedef __attribute__((ext_vector_type(4))) float f32x4;
typedef __attribute__((ext_vector_type(2))) float f32x2;
typedef __attribute__((ext_vector_type(4))) unsigned u32x4;
typedef __attribute__((ext_vector_type(2))) unsigned u32x2;
typedef unsigned short u16;

constexpr int L_ = 2048, D_ = 512, NS_ = 128, F_ = 1792;

constexpr size_t WS_W1T = 0;
constexpr size_t WS_W2T = WS_W1T + 512 * 1024;
constexpr size_t WS_W3T = WS_W2T + 512 * 1024;
constexpr size_t WS_W4T = WS_W3T + 56 * 32 * 1024;
constexpr size_t WS_CS1 = WS_W4T + 16 * 112 * 1024;
constexpr size_t WS_CS3 = WS_CS1 + 4096;
constexpr size_t WS_ST0 = WS_CS3 + 8192;
constexpr size_t WS_ST1 = WS_ST0 + 16384;
constexpr size_t WS_HP = WS_ST1 + 64 * 4 * 32 * 16;
constexpr size_t WS_H1 = WS_HP + 2 * 1024 * 1024;
constexpr size_t WS_X1 = WS_H1 + 2 * 1024 * 1024;
constexpr size_t WS_SLAB = WS_X1 + 2 * 1024 * 1024;
constexpr size_t WS_END = WS_SLAB + 16 * 1024 * 1024;

DI float bf2f(unsigned b) { return __builtin_bit_cast(float, b << 16); }
DI float bflo(unsigned w) { return __builtin_bit_cast(float, w << 16); }
DI float bfhi(unsigned w) { return __builtin_bit_cast(float, w & 0xffff0000u); }
DI float bfr(float x) { return (float)(__bf16)x; }
DI unsigned pack_bf16(float a, float b) {
  f32x2 v = {a, b};
  bf16x2 r = __builtin_convertvector(v, bf16x2);
  return __builtin_bit_cast(unsigned, r);
}
DI unsigned pack_f16(float a, float b) {
  f32x2 v = {a, b};
  f16x2 r = __builtin_convertvector(v, f16x2);
  return __builtin_bit_cast(unsigned, r);
}
DI float gelu_tanh(float x) {
  const float c0 = -2.0f * 0.7978845608028654f * 1.4426950408889634f;
  const float c1 = c0 * 0.044715f;
  float t = x * __builtin_fmaf(x * x, c1, c0);
  float e = __builtin_amdgcn_exp2f(t);
  return x * __builtin_amdgcn_rcpf(1.0f + e);
}
DI f32x16 mfma_bf16(u32x4 a, u32x4 b, f32x16 c) {
  return __builtin_amdgcn_mfma_f32_32x32x16_bf16(__builtin_bit_cast(bf16x8, a), __builtin_bit_cast(bf16x8, b), c, 0, 0, 0);
}
DI f32x16 mfma_f16(u32x4 a, u32x4 b, f32x16 c) {
  return __builtin_amdgcn_mfma_f32_32x32x16_f16(__builtin_bit_cast(f16x8, a), __builtin_bit_cast(f16x8, b), c, 0, 0, 0);
}
DI f32x4 mfma16_f16(u32x4 a, u32x4 b, f32x4 c) {
  return __builtin_amdgcn_mfma_f32_16x16x32_f16(__builtin_bit_cast(f16x8, a), __builtin_bit_cast(f16x8, b), c, 0, 0, 0);
}
DI float wave_sum(float v) {
#pragma unroll
  for (int m = 32; m >= 1; m >>= 1) v += __shfl_xor(v, m);
  return v;
}
DI float wave_max(float v) {
#pragma unroll
  for (int m = 32; m >= 1; m >>= 1) v = fmaxf(v, __shfl_xor(v, m));
  return v;
}
DI float dot2bf(unsigned a, unsigned b, float c) {
  return __builtin_amdgcn_fdot2_f32_bf16(__builtin_bit_cast(bf16x2, a), __builtin_bit_cast(bf16x2, b), c, false);
}

struct Params {
  const u16 *x, *logA, *Aim, *Cre, *Cim, *logdt, *Dskip, *w1, *w2, *w3, *w4;
  u16 *out;
  unsigned char *ws;
};

__global__ void __launch_bounds__(512, 2) k_prep(Params p) {
  __shared__ float red[8][64];
  const int tid = threadIdx.x, lane = tid & 63, w = tid >> 6, b = blockIdx.x;
  {
    const int row = 8 * b + w;
    u32x4 v = *(const u32x4 *)(p.x + (size_t)row * D_ + 8 * lane);
    float f[8];
#pragma unroll
    for (int i = 0; i < 4; ++i) { f[2 * i] = bflo(v[i]); f[2 * i + 1] = bfhi(v[i]); }
    float s = 0;
#pragma unroll
    for (int i = 0; i < 8; ++i) s += f[i];
    const float mean = wave_sum(s) * (1.0f / D_);
    float q = 0;
#pragma unroll
    for (int i = 0; i < 8; ++i) { float dlt = f[i] - mean; q += dlt * dlt; }
    const float var = wave_sum(q) * (1.0f / D_);
    const float m = bfr(mean);
    const float sd = bfr(sqrtf(bfr(bfr(var) + 1e-5f)));
    if (lane == 0) ((f32x2 *)(p.ws + WS_ST0))[row] = f32x2{m, sd};
  }
  for (int g = b * 512 + tid; g < 294912; g += 256 * 512) {
    const u16 *W; int ncols, nks, gg; bool perm; size_t dst;
    if (g < 32768) { W = p.w1; ncols = 512; nks = 32; gg = g; perm = false; dst = WS_W1T; }
    else if (g < 65536) { W = p.w2; ncols = 512; nks = 32; gg = g - 32768; perm = true; dst = WS_W2T; }
    else if (g < 65536 + 114688) { W = p.w3; ncols = 1792; nks = 32; gg = g - 65536; perm = true; dst = WS_W3T; }
    else { W = p.w4; ncols = 512; nks = 112; gg = g - 65536 - 114688; perm = true; dst = WS_W4T; }
    const int ln = gg & 63, fr = gg >> 6, s = fr % nks, mt = fr / nks;
    const int m = 32 * mt + (ln & 31), h = ln >> 5;
    unsigned v[8];
#pragma unroll
    for (int j = 0; j < 8; ++j) {
      const int k = perm ? (16 * s + 8 * (j >> 2) + 4 * h + (j & 3)) : (16 * s + 8 * h + j);
      v[j] = W[(size_t)k * ncols + m];
    }
    u32x4 o = {v[0] | (v[1] << 16), v[2] | (v[3] << 16), v[4] | (v[5] << 16), v[6] | (v[7] << 16)};
    *(u32x4 *)(p.ws + dst + (size_t)gg * 16) = o;
  }
  if (b < 36) {
    const u16 *W; int ncols, col; float *dst;
    if (b < 8) { W = p.w1; ncols = 512; col = 64 * b + lane; dst = (float *)(p.ws + WS_CS1); }
    else { W = p.w3; ncols = 1792; col = 64 * (b - 8) + lane; dst = (float *)(p.ws + WS_CS3); }
    float s = 0;
    for (int r = 0; r < 64; ++r) s += bf2f(W[(size_t)(64 * w + r) * ncols + col]);
    red[w][lane] = s;
    __syncthreads();
    if (w == 0) {
      float t = 0;
#pragma unroll
      for (int i = 0; i < 8; ++i) t += red[i][lane];
      dst[col] = t;
    }
  }
}

constexpr int CV_U = 0;
constexpr int CV_XT = 15360;
constexpr int CV_KT = CV_XT + 18432;
constexpr int CV_Z = CV_KT + 9216;
constexpr int CV_R = CV_Z, CV_L = CV_Z + 2 * 32 * 528;
constexpr int KC_STRIDE = 4416;
constexpr int CV_END = CV_Z + 67584;

struct cplx { float r, i; };
DI cplx cmul(cplx a, cplx b) { return cplx{a.r * b.r - a.i * b.i, a.r * b.i + a.i * b.r}; }
DI cplx zpow(float er, float tr, float pw) {
  const float mag = __builtin_amdgcn_exp2f(er * pw);
  const float ph = __builtin_amdgcn_fractf(tr * pw);
  return cplx{mag * __builtin_amdgcn_cosf(ph), mag * __builtin_amdgcn_sinf(ph)};
}

__global__ void __launch_bounds__(512, 2) k_conv(Params p) {
  __shared__ __attribute__((aligned(16))) unsigned char sm[CV_END];
  const int tid = threadIdx.x, lane = tid & 63, w = __builtin_amdgcn_readfirstlane(tid >> 6), b = blockIdx.x;
  const int pr = (b & 7) * 32 + (b >> 3), d0 = 2 * pr;
  const int ch = w >> 2, q = w & 3;

  unsigned xv[4]; f32x2 stv[4];
#pragma unroll
  for (int i = 0; i < 4; ++i) {
    xv[i] = *(const unsigned *)(p.x + (size_t)(4 * tid + i) * D_ + d0);
    stv[i] = ((const f32x2 *)(p.ws + WS_ST0))[4 * tid + i];
  }
  const int d = d0 + ch;
  const float dt = bfr(expf(bf2f(p.logdt[d])));
  float S;
  {
    const float c0r = bf2f(p.Cre[d * NS_ + lane]) * dt, c0i = bf2f(p.Cim[d * NS_ + lane]) * dt;
    const float c1r = bf2f(p.Cre[d * NS_ + 64 + lane]) * dt, c1i = bf2f(p.Cim[d * NS_ + 64 + lane]) * dt;
    float mx = fmaxf(fmaxf(fabsf(c0r), fabsf(c0i)), fmaxf(fabsf(c1r), fabsf(c1i)));
    mx = wave_max(mx);
    int e = 0;
    if (mx > 0.f && mx < 3.0e38f) (void)frexpf(mx, &e);
    e = e < -100 ? -100 : (e > 100 ? 100 : e);
    S = ldexpf(1.0f, -e);
    const int n = 64 * (q & 1) + lane;
    const cplx C = (q & 1) ? cplx{c1r * S, c1i * S} : cplx{c0r * S, c0i * S};
    const float Are = -bfr(expf(bf2f(p.logA[d * NS_ + n])));
    const float ar = Are * dt, ai = bf2f(p.Aim[d * NS_ + n]) * dt;
    const float er = ar * 1.4426950408889634f, tr = ai * 0.15915494309189535f;
    const int jh = q >> 1;
    unsigned *Rim = (unsigned *)(sm + CV_R + ch * 32 * 528);
    unsigned *Lim = (unsigned *)(sm + CV_L + ch * 32 * 528);
    cplx cur = zpow(er, tr, 128.0f * jh);
    const cplx step = zpow(er, tr, 8.0f);
#pragma unroll
    for (int jj = 0; jj < 16; ++jj) {
      Rim[(16 * jh + jj) * 132 + n] = pack_f16(cur.r, cur.i);
      cur = cmul(cur, step);
    }
    if (jh == 0) {
      const cplx z1 = zpow(er, tr, 1.0f), z256 = zpow(er, tr, 256.0f);
      cplx g = C;
#pragma unroll
      for (int r = 0; r < 8; ++r) {
        Lim[r * 132 + n] = pack_f16(g.r, -g.i);
        if ((r & 1) == 0) { cplx t = cmul(g, z256); Lim[(8 + (r >> 1)) * 132 + n] = pack_f16(t.r, -t.i); }
        g = cmul(g, z1);
      }
    } else {
      const cplx g4 = cmul(C, zpow(er, tr, 4.0f));
      const cplx z512 = zpow(er, tr, 512.0f), z768 = zpow(er, tr, 768.0f);
      cplx t;
      t = cmul(C, z512);  Lim[12 * 132 + n] = pack_f16(t.r, -t.i);
      t = cmul(g4, z512); Lim[13 * 132 + n] = pack_f16(t.r, -t.i);
      t = cmul(C, z768);  Lim[14 * 132 + n] = pack_f16(t.r, -t.i);
      t = cmul(g4, z768); Lim[15 * 132 + n] = pack_f16(t.r, -t.i);
#pragma unroll
      for (int i = 0; i < 5; ++i) {
        t = cmul(C, zpow(er, tr, 1024.0f + 256.0f * i));
        Lim[(16 + i) * 132 + n] = pack_f16(t.r, -t.i);
      }
    }
  }
  {
    const float D0 = bf2f(p.Dskip[d0]), D1 = bf2f(p.Dskip[d0 + 1]);
    unsigned u0[2], u1[2]; u32x4 xt0, xt1;
    float uu0[4], uu1[4];
#pragma unroll
    for (int i = 0; i < 4; ++i) {
      const float x0 = bflo(xv[i]), x1 = bfhi(xv[i]);
      uu0[i] = bfr(bfr(x0 - stv[i][0]) / stv[i][1]);
      uu1[i] = bfr(bfr(x1 - stv[i][0]) / stv[i][1]);
      xt0[i] = (xv[i] & 0xffffu) | (pack_bf16(0.f, uu0[i] * D0) & 0xffff0000u);
      xt1[i] = (xv[i] >> 16) | (pack_bf16(0.f, uu1[i] * D1) & 0xffff0000u);
    }
    u0[0] = pack_f16(uu0[0], uu0[1]); u0[1] = pack_f16(uu0[2], uu0[3]);
    u1[0] = pack_f16(uu1[0], uu1[1]); u1[1] = pack_f16(uu1[2], uu1[3]);
    const int l0 = 4 * tid, a = l0 >> 5, bb = l0 & 31;
    *(u32x2 *)(sm + CV_U + ((a + 32) * 40 + bb) * 2) = u32x2{u0[0], u0[1]};
    *(u32x2 *)(sm + CV_U + 7680 + ((a + 32) * 40 + bb) * 2) = u32x2{u1[0], u1[1]};
    *(u32x4 *)(sm + CV_XT + (l0 + 4 * a) * 4) = xt0;
    *(u32x4 *)(sm + CV_XT + 9216 + (l0 + 4 * a) * 4) = xt1;
    if (tid < 320) {
      const int c2 = tid / 160, o = tid % 160;
      *(u32x4 *)(sm + CV_U + c2 * 7680 + o * 16) = u32x4{0, 0, 0, 0};
    }
  }
  __syncthreads();
  {
    const unsigned char *Rb = sm + CV_R + ch * 32 * 528 + (16 * (q >> 1) + (lane & 15)) * 528 + (lane >> 4) * 16;
    const unsigned char *Lb = sm + CV_L + ch * 32 * 528 + (16 * (q & 1) + (lane & 15)) * 528 + (lane >> 4) * 16;
    f32x4 acc = {0, 0, 0, 0};
#pragma unroll
    for (int s = 0; s < 8; ++s) acc = mfma16_f16(*(const u32x4 *)(Rb + s * 64), *(const u32x4 *)(Lb + s * 64), acc);
    f32x4 o = acc * 2.0f;
    *(f32x4 *)(sm + CV_KT + ch * 4608 + ((16 * (q & 1) + (lane & 15)) * 36 + 16 * (q >> 1) + 4 * (lane >> 4)) * 4) = o;
  }
  __syncthreads();
  for (int t = tid; t < 2 * 521; t += 512) {
    const int c2 = t / 521, g = t % 521;
    const float *Kt = (const float *)(sm + CV_KT + c2 * 4608);
    float v[4];
#pragma unroll
    for (int i = 0; i < 4; ++i) {
      const int tau = 2048 - (4 * g + i);
      float val = 0.f;
      if (tau >= 0 && tau < 2048) {
        const int P = (int)bfr((float)tau);
        int col, j;
        if (P < 256) { col = P & 7; j = P >> 3; }
        else if (P < 512) { col = 8 + ((P & 7) >> 1); j = (P - 256) >> 3; }
        else if (P < 1024) { col = 12 + 2 * ((P - 512) >> 8) + ((P & 7) >> 2); j = (P & 255) >> 3; }
        else if (P < 2048) { col = 16 + ((P - 1024) >> 8); j = (P & 255) >> 3; }
        else { col = 20; j = 0; }
        val = Kt[col * 36 + j];
      }
      v[i] = val;
    }
    *(u32x2 *)(sm + CV_Z + c2 * 4 * KC_STRIDE + g * 8) = u32x2{pack_f16(v[0], v[1]), pack_f16(v[2], v[3])};
  }
  __syncthreads();
  for (int t = tid; t < 2 * 521; t += 512) {
    const int c2 = t / 521, g = t % 521;
    unsigned char *base = sm + CV_Z + c2 * 4 * KC_STRIDE;
    const u32x2 cur = *(const u32x2 *)(base + g * 8);
    u32x2 prev = {0, 0};
    if (g > 0) prev = *(const u32x2 *)(base + g * 8 - 8);
    u32x2 o1 = {__builtin_amdgcn_alignbit(cur[0], prev[1], 16), __builtin_amdgcn_alignbit(cur[1], cur[0], 16)};
    u32x2 o2 = {prev[1], cur[0]};
    u32x2 o3 = {__builtin_amdgcn_alignbit(prev[1], prev[0], 16), __builtin_amdgcn_alignbit(cur[0], prev[1], 16)};
    *(u32x2 *)(base + 1 * KC_STRIDE + g * 8) = o1;
    *(u32x2 *)(base + 2 * KC_STRIDE + g * 8) = o2;
    *(u32x2 *)(base + 3 * KC_STRIDE + g * 8) = o3;
  }
  __syncthreads();
  f32x16 acc0, acc1;
#pragma unroll
  for (int i = 0; i < 16; ++i) { acc0[i] = 0.f; acc1[i] = 0.f; }
  {
    const int m = lane & 31, h = lane >> 5, c = m & 3, mu = m >> 2;
    const unsigned char *Ab = sm + CV_Z + ch * 4 * KC_STRIDE + c * KC_STRIDE + (2048 - 4 * mu + 8 * h) * 2;
    const unsigned char *Bb = sm + CV_U + ch * 7680 + ((m + 32) * 40 + 8 * h) * 2;
#pragma unroll
    for (int dd = 0; dd < 8; ++dd) {
      const int dl = 8 * q + dd;
#pragma unroll
      for (int s = 0; s < 2; ++s) {
        const int e0 = 2 * dl - s, e1 = e0 + 64;
        const u32x2 a0l = *(const u32x2 *)(Ab - 32 * e0), a0h = *(const u32x2 *)(Ab - 32 * e0 + 8);
        const u32x2 a1l = *(const u32x2 *)(Ab - 32 * e1), a1h = *(const u32x2 *)(Ab - 32 * e1 + 8);
        const u32x4 b0 = *(const u32x4 *)(Bb + (-dl) * 80 + s * 32);
        const u32x4 b1 = *(const u32x4 *)(Bb + (32 - dl) * 80 + s * 32);
        const u32x4 a0 = {a0l[0], a0l[1], a0h[0], a0h[1]};
        const u32x4 a1 = {a1l[0], a1l[1], a1h[0], a1h[1]};
        acc0 = mfma_f16(a0, b0, acc0);
        acc1 = mfma_f16(a0, b1, acc1);
        acc1 = mfma_f16(a1, b0, acc1);
      }
    }
  }
  __syncthreads();
  {
    float *red = (float *)(sm + CV_Z);
#pragma unroll
    for (int k = 0; k < 4; ++k) {
      if (k == q) continue;
      const f32x16 &a = (k >> 1) ? acc1 : acc0;
      const int o = 8 * (k & 1);
      float *dst = red + (((ch * 4 + k) * 4 + q) * 64 + lane) * 8;
      float *dstA = red + ((((ch * 4 + k) * 4 + q) * 2 + 0) * 64 + lane) * 4;
      float *dstB = red + ((((ch * 4 + k) * 4 + q) * 2 + 1) * 64 + lane) * 4;
      (void)dst;
      *(f32x4 *)dstA = f32x4{a[o], a[o + 1], a[o + 2], a[o + 3]};
      *(f32x4 *)dstB = f32x4{a[o + 4], a[o + 5], a[o + 6], a[o + 7]};
    }
  }
  __syncthreads();
  {
    const float *red = (const float *)(sm + CV_Z);
    float y[8];
    const int o = 8 * (q & 1);
#pragma unroll
    for (int i = 0; i < 8; ++i) y[i] = (q >> 1) ? acc1[o + i] : acc0[o + i];
#pragma unroll
    for (int k = 0; k < 4; ++k) {
      if (k == q) continue;
      const f32x4 va = *(const f32x4 *)(red + ((((ch * 4 + q) * 4 + k) * 2 + 0) * 64 + lane) * 4);
      const f32x4 vb = *(const f32x4 *)(red + ((((ch * 4 + q) * 4 + k) * 2 + 1) * 64 + lane) * 4);
#pragma unroll
      for (int i = 0; i < 4; ++i) { y[i] += va[i]; y[4 + i] += vb[i]; }
    }
    const float invS = 1.0f / S;
    const int n = lane & 31, h = lane >> 5, t = q >> 1, rh = q & 1;
    const int a = 32 * t + n;
    u16 *Hout = (u16 *)(sm + CV_KT) + ch * 2048;
#pragma unroll
    for (int g = 0; g < 2; ++g) {
      const int bb = 16 * rh + 8 * g + 4 * h;
      const int l0 = 32 * a + bb;
      const u32x4 xt = *(const u32x4 *)(sm + CV_XT + ch * 9216 + (l0 + 4 * a) * 4);
      float hv[4];
#pragma unroll
      for (int i = 0; i < 4; ++i) {
        const float yy = bfr(y[4 * g + i] * invS);
        hv[i] = bflo(xt[i]) + bfr(yy + bfhi(xt[i]));
      }
      *(u32x2 *)(Hout + l0) = u32x2{pack_bf16(hv[0], hv[1]), pack_bf16(hv[2], hv[3])};
    }
  }
  __syncthreads();
  {
    const u16 *Hout = (const u16 *)(sm + CV_KT);
    const u32x2 a = *(const u32x2 *)(Hout + 4 * tid), c = *(const u32x2 *)(Hout + 2048 + 4 * tid);
    u32x4 o;
    o[0] = (a[0] & 0xffffu) | (c[0] << 16);
    o[1] = (a[0] >> 16) | (c[0] & 0xffff0000u);
    o[2] = (a[1] & 0xffffu) | (c[1] << 16);
    o[3] = (a[1] >> 16) | (c[1] & 0xffff0000u);
    *(u32x4 *)(p.ws + WS_HP + ((size_t)pr * 2048 + 4 * tid) * 4) = o;
  }
}

DI void team_map(int b, int &T, int &j) { T = 4 * (b & 7) + ((b >> 3) >> 3); j = (b >> 3) & 7; }

__global__ void __launch_bounds__(512, 2) k_g1(Params p) {
  __shared__ __attribute__((aligned(16))) unsigned char sm[32768 + 8192 + 16384];
  const int tid = threadIdx.x, lane = tid & 63, w = __builtin_amdgcn_readfirstlane(tid >> 6);
  int T, j; team_map(blockIdx.x, T, j);
  const int tt = 2 * T + (j >> 2), c = j & 3;
  const int t = lane & 31, h = lane >> 5;
  const int sh = (c == 0) ? 1 : (c == 1) ? -1 : (c == 2) ? 2 : -2;
  const int tok = 32 * tt - sh + t;
  const bool valid = tok >= 0 && tok < L_;
  const int tokc = valid ? tok : 0;
  const int mi = w & 3, kh = w >> 2;
  u32x4 A[16];
  {
    const u32x4 *wp = (const u32x4 *)(p.ws + WS_W1T) + ((size_t)((4 * c + mi) * 32 + 16 * kh) * 64 + lane);
#pragma unroll
    for (int s = 0; s < 16; ++s) A[s] = wp[s * 64];
  }
  {
    const unsigned *hp = (const unsigned *)(p.ws + WS_HP);
    float s1 = 0.f, s2 = 0.f;
#pragma unroll
    for (int ks = 0; ks < 4; ++ks) {
      const int s = 4 * w + ks;
      u32x4 v;
#pragma unroll
      for (int i = 0; i < 4; ++i) v[i] = hp[(size_t)(8 * s + 4 * h + i) * 2048 + tokc];
#pragma unroll
      for (int i = 0; i < 4; ++i) { s1 = dot2bf(v[i], 0x3f803f80u, s1); s2 = dot2bf(v[i], v[i], s2); }
      *(u32x4 *)(sm + (s * 64 + lane) * 16) = v;
    }
    *(f32x2 *)(sm + 32768 + (w * 64 + lane) * 8) = f32x2{s1, s2};
  }
  __syncthreads();
  f32x16 acc;
#pragma unroll
  for (int i = 0; i < 16; ++i) acc[i] = 0.f;
#pragma unroll
  for (int s = 0; s < 16; ++s) acc = mfma_bf16(A[s], *(const u32x4 *)(sm + ((16 * kh + s) * 64 + lane) * 16), acc);
  float s1 = 0.f, s2 = 0.f;
#pragma unroll
  for (int i = 0; i < 16; ++i) { const f32x2 v = *(const f32x2 *)(sm + 32768 + (i * 32 + t) * 8); s1 += v[0]; s2 += v[1]; }
  const float mean = s1 * (1.0f / D_);
  const float var = fmaxf(s2 * (1.0f / D_) - mean * mean, 0.f);
  const float m1 = bfr(mean), sd1 = bfr(sqrtf(bfr(bfr(var) + 1e-5f)));
  const float mean2 = (mean - m1) / sd1, var2 = var / (sd1 * sd1);
  const float m2 = bfr(mean2), sd2 = bfr(sqrtf(bfr(bfr(var2) + 1e-5f)));
  const float alpha = 1.0f / (sd1 * sd2), beta = -(m1 * alpha + m2 / sd2);
  if (w == 0 && h == 0) ((f32x4 *)(p.ws + WS_ST1))[(tt * 4 + c) * 32 + t] = f32x4{m1, sd1, 0.f, 0.f};
  {
    float *ex = (float *)(sm + 32768 + 8192);
    const int o = 8 * (1 - kh);
    *(f32x4 *)(ex + ((w * 2 + 0) * 64 + lane) * 4) = kh ? f32x4{acc[0], acc[1], acc[2], acc[3]} : f32x4{acc[8], acc[9], acc[10], acc[11]};
    *(f32x4 *)(ex + ((w * 2 + 1) * 64 + lane) * 4) = kh ? f32x4{acc[4], acc[5], acc[6], acc[7]} : f32x4{acc[12], acc[13], acc[14], acc[15]};
    (void)o;
  }
  __syncthreads();
  {
    const float *ex = (const float *)(sm + 32768 + 8192);
    const int pw = w ^ 4;
    const f32x4 va = *(const f32x4 *)(ex + ((pw * 2 + 0) * 64 + lane) * 4);
    const f32x4 vb = *(const f32x4 *)(ex + ((pw * 2 + 1) * 64 + lane) * 4);
    float v[8];
#pragma unroll
    for (int i = 0; i < 4; ++i) { v[i] = (kh ? acc[8 + i] : acc[i]) + va[i]; v[4 + i] = (kh ? acc[12 + i] : acc[4 + i]) + vb[i]; }
    const float *cs = (const float *)(p.ws + WS_CS1) + 128 * c + 32 * mi + 16 * kh + 4 * h;
    const f32x4 ca = *(const f32x4 *)cs, cb = *(const f32x4 *)(cs + 8);
    float o[8];
#pragma unroll
    for (int i = 0; i < 4; ++i) {
      o[i] = gelu_tanh(bfr(alpha * v[i] + beta * ca[i]));
      o[4 + i] = gelu_tanh(bfr(alpha * v[4 + i] + beta * cb[i]));
    }
    u32x4 pk = {pack_bf16(o[0], o[1]), pack_bf16(o[2], o[3]), pack_bf16(o[4], o[5]), pack_bf16(o[6], o[7])};
    if (!valid) pk = u32x4{0, 0, 0, 0};
    const int kstep = 8 * c + 2 * mi + kh;
    *(u32x4 *)(p.ws + WS_H1 + ((size_t)(2 * kstep + h) * 2048 + 32 * tt + t) * 16) = pk;
  }
}

__global__ void __launch_bounds__(512, 2) k_g2(Params p) {
  __shared__ __attribute__((aligned(16))) unsigned char sm[32768 + 16384];
  const int tid = threadIdx.x, lane = tid & 63, w = __builtin_amdgcn_readfirstlane(tid >> 6);
  int T, j; team_map(blockIdx.x, T, j);
  const int tt = 2 * T + (j >> 2), c = j & 3;
  const int t = lane & 31, h = lane >> 5, tok = 32 * tt + t;
  const int mi = w & 3, kh = w >> 2;
  u32x4 A[16];
  {
    const u32x4 *wp = (const u32x4 *)(p.ws + WS_W2T) + ((size_t)((4 * c + mi) * 32 + 16 * kh) * 64 + lane);
#pragma unroll
    for (int s = 0; s < 16; ++s) A[s] = wp[s * 64];
  }
#pragma unroll
  for (int ks = 0; ks < 4; ++ks) {
    const int s = 4 * w + ks;
    const u32x4 v = *(const u32x4 *)(p.ws + WS_H1 + ((size_t)(2 * s + h) * 2048 + tok) * 16);
    *(u32x4 *)(sm + (s * 64 + lane) * 16) = v;
  }
  const int f0 = 128 * c + 32 * mi + 16 * kh + 4 * h;
  unsigned hv[4];
  {
    const unsigned *hp = (const unsigned *)(p.ws + WS_HP);
    hv[0] = hp[(size_t)(f0 / 2) * 2048 + tok];
    hv[1] = hp[(size_t)(f0 / 2 + 1) * 2048 + tok];
    hv[2] = hp[(size_t)(f0 / 2 + 4) * 2048 + tok];
    hv[3] = hp[(size_t)(f0 / 2 + 5) * 2048 + tok];
  }
  const f32x4 st = (t < 31) ? ((const f32x4 *)(p.ws + WS_ST1))[(tt * 4 + 0) * 32 + t + 1] : ((const f32x4 *)(p.ws + WS_ST1))[(tt * 4 + 1) * 32 + 30];
  __syncthreads();
  f32x16 acc;
#pragma unroll
  for (int i = 0; i < 16; ++i) acc[i] = 0.f;
#pragma unroll
  for (int s = 0; s < 16; ++s) acc = mfma_bf16(A[s], *(const u32x4 *)(sm + ((16 * kh + s) * 64 + lane) * 16), acc);
  {
    float *ex = (float *)(sm + 32768);
    *(f32x4 *)(ex + ((w * 2 + 0) * 64 + lane) * 4) = kh ? f32x4{acc[0], acc[1], acc[2], acc[3]} : f32x4{acc[8], acc[9], acc[10], acc[11]};
    *(f32x4 *)(ex + ((w * 2 + 1) * 64 + lane) * 4) = kh ? f32x4{acc[4], acc[5], acc[6], acc[7]} : f32x4{acc[12], acc[13], acc[14], acc[15]};
  }
  __syncthreads();
  {
    const float *ex = (const float *)(sm + 32768);
    const int pw = w ^ 4;
    const f32x4 va = *(const f32x4 *)(ex + ((pw * 2 + 0) * 64 + lane) * 4);
    const f32x4 vb = *(const f32x4 *)(ex + ((pw * 2 + 1) * 64 + lane) * 4);
    float v[8];
#pragma unroll
    for (int i = 0; i < 4; ++i) { v[i] = (kh ? acc[8 + i] : acc[i]) + va[i]; v[4 + i] = (kh ? acc[12 + i] : acc[4 + i]) + vb[i]; }
    float o[8];
    const float m1 = st[0], sd1 = st[1];
#pragma unroll
    for (int i = 0; i < 4; ++i) {
      const float h0 = bflo(hv[i]), h1 = bfhi(hv[i]);
      o[2 * i] = bfr(bfr(h0 - m1) / sd1) + bfr(v[2 * i]);
      o[2 * i + 1] = bfr(bfr(h1 - m1) / sd1) + bfr(v[2 * i + 1]);
    }
    u32x4 pk = {pack_bf16(o[0], o[1]), pack_bf16(o[2], o[3]), pack_bf16(o[4], o[5]), pack_bf16(o[6], o[7])};
    const int kstep = 8 * c + 2 * mi + kh;
    *(u32x4 *)(p.ws + WS_X1 + ((size_t)(2 * kstep + h) * 2048 + tok) * 16) = pk;
  }
}

__global__ void __launch_bounds__(512, 2) k_g34(Params p) {
  __shared__ __attribute__((aligned(16))) unsigned char sm[65536 + 28672 + 8192];
  const int tid = threadIdx.x, lane = tid & 63, w = __builtin_amdgcn_readfirstlane(tid >> 6);
  int T, j; team_map(blockIdx.x, T, j);
  const int t = lane & 31, h = lane >> 5;
  u32x4 A[32];
  const int w7 = w < 7 ? w : 6;
  {
    const u32x4 *wp = (const u32x4 *)(p.ws + WS_W3T) + ((size_t)((7 * j + w7) * 32) * 64 + lane);
#pragma unroll
    for (int s = 0; s < 32; ++s) A[s] = wp[s * 64];
  }
  {
    const int tl = w >> 2;
    float s1 = 0.f, s2 = 0.f;
#pragma unroll
    for (int ks = 0; ks < 8; ++ks) {
      const int s = 8 * (w & 3) + ks;
      const u32x4 v = *(const u32x4 *)(p.ws + WS_X1 + ((size_t)(2 * s + h) * 2048 + 64 * T + 32 * tl + t) * 16);
#pragma unroll
      for (int i = 0; i < 4; ++i) { s1 = dot2bf(v[i], 0x3f803f80u, s1); s2 = dot2bf(v[i], v[i], s2); }
      *(u32x4 *)(sm + ((tl * 32 + s) * 64 + lane) * 16) = v;
    }
    *(f32x2 *)(sm + 65536 + 28672 + (w * 64 + lane) * 8) = f32x2{s1, s2};
  }
  __syncthreads();
  f32x16 acc0, acc1;
#pragma unroll
  for (int i = 0; i < 16; ++i) { acc0[i] = 0.f; acc1[i] = 0.f; }
  if (w < 7) {
#pragma unroll
    for (int s = 0; s < 32; ++s) {
      acc0 = mfma_bf16(A[s], *(const u32x4 *)(sm + ((0 * 32 + s) * 64 + lane) * 16), acc0);
      acc1 = mfma_bf16(A[s], *(const u32x4 *)(sm + ((1 * 32 + s) * 64 + lane) * 16), acc1);
    }
  }
  u32x4 A4[28];
  {
    const u32x4 *wp = (const u32x4 *)(p.ws + WS_W4T) + ((size_t)((2 * w) * 112 + 14 * j) * 64 + lane);
#pragma unroll
    for (int s = 0; s < 14; ++s) { A4[s] = wp[s * 64]; A4[14 + s] = wp[(112 + s) * 64]; }
  }
  if (w < 7) {
    float r3[2], mr3[2];
#pragma unroll
    for (int tl = 0; tl < 2; ++tl) {
      float s1 = 0.f, s2 = 0.f;
#pragma unroll
      for (int i = 0; i < 8; ++i) {
        const f32x2 v = *(const f32x2 *)(sm + 65536 + 28672 + ((tl * 4 + (i >> 1)) * 64 + (i & 1) * 32 + t) * 8);
        s1 += v[0]; s2 += v[1];
      }
      const float mean = s1 * (1.0f / D_);
      const float var = fmaxf(s2 * (1.0f / D_) - mean * mean, 0.f);
      const float m3 = bfr(mean), sd3 = bfr(sqrtf(bfr(bfr(var) + 1e-5f)));
      r3[tl] = 1.0f / sd3; mr3[tl] = -m3 * r3[tl];
    }
    const float *cs = (const float *)(p.ws + WS_CS3) + 32 * (7 * j + w7) + 4 * h;
    f32x4 cv[4];
#pragma unroll
    for (int g = 0; g < 4; ++g) cv[g] = *(const f32x4 *)(cs + 8 * g);
#pragma unroll
    for (int tl = 0; tl < 2; ++tl) {
      const f32x16 &a = tl ? acc1 : acc0;
      float o[16];
#pragma unroll
      for (int i = 0; i < 16; ++i) o[i] = gelu_tanh(bfr(r3[tl] * a[i] + mr3[tl] * cv[i >> 2][i & 3]));
#pragma unroll
      for (int s = 0; s < 2; ++s) {
        u32x4 pk = {pack_bf16(o[8 * s], o[8 * s + 1]), pack_bf16(o[8 * s + 2], o[8 * s + 3]), pack_bf16(o[8 * s + 4], o[8 * s + 5]), pack_bf16(o[8 * s + 6], o[8 * s + 7])};
        *(u32x4 *)(sm + 65536 + ((tl * 14 + 2 * w + s) * 64 + lane) * 16) = pk;
      }
    }
  }
  __syncthreads();
  f32x16 o00, o01, o10, o11;
#pragma unroll
  for (int i = 0; i < 16; ++i) { o00[i] = 0.f; o01[i] = 0.f; o10[i] = 0.f; o11[i] = 0.f; }
#pragma unroll
  for (int s = 0; s < 14; ++s) {
    const u32x4 b0 = *(const u32x4 *)(sm + 65536 + ((0 * 14 + s) * 64 + lane) * 16);
    const u32x4 b1 = *(const u32x4 *)(sm + 65536 + ((1 * 14 + s) * 64 + lane) * 16);
    o00 = mfma_bf16(A4[s], b0, o00);
    o01 = mfma_bf16(A4[s], b1, o01);
    o10 = mfma_bf16(A4[14 + s], b0, o10);
    o11 = mfma_bf16(A4[14 + s], b1, o11);
  }
  {
    unsigned char *slab = p.ws + WS_SLAB + (size_t)j * 2 * 1024 * 1024;
#pragma unroll
    for (int mt = 0; mt < 2; ++mt)
#pragma unroll
      for (int tl = 0; tl < 2; ++tl) {
        const f32x16 &a = mt ? (tl ? o11 : o10) : (tl ? o01 : o00);
#pragma unroll
        for (int s = 0; s < 2; ++s) {
          u32x4 pk = {pack_bf16(a[8 * s], a[8 * s + 1]), pack_bf16(a[8 * s + 2], a[8 * s + 3]), pack_bf16(a[8 * s + 4], a[8 * s + 5]), pack_bf16(a[8 * s + 6], a[8 * s + 7])};
          const int kstep = 2 * (2 * w + mt) + s;
          *(u32x4 *)(slab + ((size_t)(2 * kstep + h) * 2048 + 64 * T + 32 * tl + t) * 16) = pk;
        }
      }
  }
}

__global__ void __launch_bounds__(512, 2) k_fin(Params p) {
  const int tid = threadIdx.x;
  int T, j; team_map(blockIdx.x, T, j);
  const int h = tid & 1, ks = (tid >> 1) & 3, tk = tid >> 3;
  const int tok = 64 * T + tk, kstep = 4 * j + ks, chunk = 2 * kstep + h;
  float acc[8];
#pragma unroll
  for (int i = 0; i < 8; ++i) acc[i] = 0.f;
  u32x4 sl[8];
#pragma unroll
  for (int q = 0; q < 8; ++q) sl[q] = *(const u32x4 *)(p.ws + WS_SLAB + (size_t)q * 2 * 1024 * 1024 + ((size_t)chunk * 2048 + tok) * 16);
  const u32x4 x1 = *(const u32x4 *)(p.ws + WS_X1 + ((size_t)chunk * 2048 + tok) * 16);
  const int f0 = 16 * kstep + 4 * h;
  const unsigned *hp = (const unsigned *)(p.ws + WS_HP);
  unsigned hv[4];
  hv[0] = hp[(size_t)(f0 / 2) * 2048 + tok];
  hv[1] = hp[(size_t)(f0 / 2 + 1) * 2048 + tok];
  hv[2] = hp[(size_t)(f0 / 2 + 4) * 2048 + tok];
  hv[3] = hp[(size_t)(f0 / 2 + 5) * 2048 + tok];
#pragma unroll
  for (int q = 0; q < 8; ++q)
#pragma unroll
    for (int i = 0; i < 4; ++i) { acc[2 * i] += bflo(sl[q][i]); acc[2 * i + 1] += bfhi(sl[q][i]); }
  float o[8];
#pragma unroll
  for (int i = 0; i < 4; ++i) {
    o[2 * i] = bflo(hv[i]) + bfr(bflo(x1[i]) + bfr(acc[2 * i]));
    o[2 * i + 1] = bfhi(hv[i]) + bfr(bfhi(x1[i]) + bfr(acc[2 * i + 1]));
  }
  u16 *dst = p.out + (size_t)tok * D_ + f0;
  *(u32x2 *)dst = u32x2{pack_bf16(o[0], o[1]), pack_bf16(o[2], o[3])};
  *(u32x2 *)(dst + 8) = u32x2{pack_bf16(o[4], o[5]), pack_bf16(o[6], o[7])};
}

extern "C" void kernel_launch(void *const *d_in, const int *in_sizes, int n_in, void *d_out, int out_size, void *d_ws,
                              size_t ws_size, hipStream_t stream) {
  Params p;
  p.x = (const u16 *)d_in[0];
  p.logA = (const u16 *)d_in[1];
  p.Aim = (const u16 *)d_in[2];
  p.Cre = (const u16 *)d_in[3];
  p.Cim = (const u16 *)d_in[4];
  p.logdt = (const u16 *)d_in[5];
  p.Dskip = (const u16 *)d_in[6];
  p.w1 = (const u16 *)d_in[8];
  p.w2 = (const u16 *)d_in[9];
  p.w3 = (const u16 *)d_in[10];
  p.w4 = (const u16 *)d_in[11];
  p.out = (u16 *)d_out;
  p.ws = (unsigned char *)d_ws;
  k_prep<<<256, 512, 0, stream>>>(p);
  k_conv<<<256, 512, 0, stream>>>(p);
  k_g1<<<256, 512, 0, stream>>>(p);
  k_g2<<<256, 512, 0, stream>>>(p);
  k_g34<<<256, 512, 0, stream>>>(p);
  k_fin<<<256, 512, 0, stream>>>(p);
}
```

```cpp
#include <hip/hip_runtime.h>
#include <hip/hip_bf16.h>
#include <stdint.h>

#define DI __device__ __forceinline__
typedef __attribute__((ext_vector_type(8))) short bf16x8;
typedef __attribute__((ext_vector_type(8))) _Float16 f16x8;
typedef __attribute__((ext_vector_type(2))) _Float16 f16x2;
typedef __attribute__((ext_vector_type(2))) __bf16 bf16x2;
typedef __attribute__((ext_vector_type(16))) float f32x16;
typedef __attribute__((ext_vector_type(4))) float f32x4;
typedef __attribute__((ext_vector_type(2))) float f32x2;
typedef __attribute__((ext_vector_type(4))) unsigned u32x4;
typedef __attribute__((ext_vector_type(2))) unsigned u32x2;
typedef unsigned short u16;

constexpr int L_ = 2048, D_ = 512, NS_ = 128, F_ = 1792;

constexpr size_t WS_W1T = 0;
constexpr size_t WS_W2T = WS_W1T + 512 * 1024;
constexpr size_t WS_W3T = WS_W2T + 512 * 1024;
constexpr size_t WS_W4T = WS_W3T + 56 * 32 * 1024;
constexpr size_t WS_CS1 = WS_W4T + 16 * 112 * 1024;
constexpr size_t WS_CS3 = WS_CS1 + 4096;
constexpr size_t WS_ST0 = WS_CS3 + 8192;
constexpr size_t WS_ST1 = WS_ST0 + 16384;
constexpr size_t WS_HP = WS_ST1 + 64 * 4 * 32 * 16;
constexpr size_t WS_H1 = WS_HP + 2 * 1024 * 1024;
constexpr size_t WS_X1 = WS_H1 + 2 * 1024 * 1024;
constexpr size_t WS_SLAB = WS_X1 + 2 * 1024 * 1024;
constexpr size_t WS_END = WS_SLAB + 16 * 1024 * 1024;

DI float bf2f(unsigned b) { return __builtin_bit_cast(float, b << 16); }
DI float bflo(unsigned w) { return __builtin_bit_cast(float, w << 16); }
DI float bfhi(unsigned w) { return __builtin_bit_cast(float, w & 0xffff0000u); }
DI float bfr(float x) { return (float)(__bf16)x; }
DI unsigned pack_bf16(float a, float b) {
  f32x2 v = {a, b};
  bf16x2 r = __builtin_convertvector(v, bf16x2);
  return __builtin_bit_cast(unsigned, r);
}
DI unsigned pack_f16(float a, float b) {
  f32x2 v = {a, b};
  f16x2 r = __builtin_convertvector(v, f16x2);
  return __builtin_bit_cast(unsigned, r);
}
DI float gelu_tanh(float x) {
  const float c0 = -2.0f * 0.7978845608028654f * 1.4426950408889634f;
  const float c1 = c0 * 0.044715f;
  float t = x * __builtin_fmaf(x * x, c1, c0);
  float e = __builtin_amdgcn_exp2f(t);
  return x * __builtin_amdgcn_rcpf(1.0f + e);
}
DI f32x16 mfma_bf16(u32x4 a, u32x4 b, f32x16 c) {
  return __builtin_amdgcn_mfma_f32_32x32x16_bf16(__builtin_bit_cast(bf16x8, a), __builtin_bit_cast(bf16x8, b), c, 0, 0, 0);
}
DI f32x16 mfma_f16(u32x4 a, u32x4 b, f32x16 c) {
  return __builtin_amdgcn_mfma_f32_32x32x16_f16(__builtin_bit_cast(f16x8, a), __builtin_bit_cast(f16x8, b), c, 0, 0, 0);
}
DI f32x4 mfma16_f16(u32x4 a, u32x4 b, f32x4 c) {
  return __builtin_amdgcn_mfma_f32_16x16x32_f16(__builtin_bit_cast(f16x8, a), __builtin_bit_cast(f16x8, b), c, 0, 0, 0);
}
DI float wave_sum(float v) {
#pragma unroll
  for (int m = 32; m >= 1; m >>= 1) v += __shfl_xor(v, m);
  return v;
}
DI float wave_max(float v) {
#pragma unroll
  for (int m = 32; m >= 1; m >>= 1) v = fmaxf(v, __shfl_xor(v, m));
  return v;
}
DI float dot2bf(unsigned a, unsigned b, float c) {
  return __builtin_amdgcn_fdot2_f32_bf16(__builtin_bit_cast(bf16x2, a), __builtin_bit_cast(bf16x2, b), c, false);
}

struct Params {
  const u16 *x, *logA, *Aim, *Cre, *Cim, *logdt, *Dskip, *w1, *w2, *w3, *w4;
  u16 *out;
  unsigned char *ws;
};

__global__ void __launch_bounds__(512, 2) k_prep(Params p) {
  __shared__ float red[8][64];
  const int tid = threadIdx.x, lane = tid & 63, w = tid >> 6, b = blockIdx.x;
  if (w == 7) {
    unsigned long long pc; unsigned len;
    asm volatile("s_getpc_b64 %0\n.Lpf_here%=:\n\ts_mov_b32 %1, text_end_marker-.Lpf_here%=" : "=s"(pc), "=s"(len));
    const __attribute__((address_space(1))) unsigned *cp = (const __attribute__((address_space(1))) unsigned *)pc;
    unsigned a = 0;
#pragma unroll
    for (int i = 0; i < 6; ++i) { unsigned off = (i * 64 + lane) * 128; off = off < len - 4 ? off : len - 4; a += cp[off >> 2]; }
    asm volatile("" :: "v"(a));
  }
  {
    const int row = 8 * b + w;
    u32x4 v = *(const u32x4 *)(p.x + (size_t)row * D_ + 8 * lane);
    float f[8];
#pragma unroll
    for (int i = 0; i < 4; ++i) { f[2 * i] = bflo(v[i]); f[2 * i + 1] = bfhi(v[i]); }
    float s = 0;
#pragma unroll
    for (int i = 0; i < 8; ++i) s += f[i];
    const float mean = wave_sum(s) * (1.0f / D_);
    float q = 0;
#pragma unroll
    for (int i = 0; i < 8; ++i) { float dlt = f[i] - mean; q += dlt * dlt; }
    const float var = wave_sum(q) * (1.0f / D_);
    const float m = bfr(mean);
    const float sd = bfr(sqrtf(bfr(bfr(var) + 1e-5f)));
    if (lane == 0) ((f32x2 *)(p.ws + WS_ST0))[row] = f32x2{m, sd};
  }
  for (int g = b * 512 + tid; g < 294912; g += 256 * 512) {
    const u16 *W; int ncols, nks, gg; bool perm; size_t dst;
    if (g < 32768) { W = p.w1; ncols = 512; nks = 32; gg = g; perm = false; dst = WS_W1T; }
    else if (g < 65536) { W = p.w2; ncols = 512; nks = 32; gg = g - 32768; perm = true; dst = WS_W2T; }
    else if (g < 65536 + 114688) { W = p.w3; ncols = 1792; nks = 32; gg = g - 65536; perm = true; dst = WS_W3T; }
    else { W = p.w4; ncols = 512; nks = 112; gg = g - 65536 - 114688; perm = true; dst = WS_W4T; }
    const int ln = gg & 63, fr = gg >> 6, s = fr % nks, mt = fr / nks;
    const int m = 32 * mt + (ln & 31), h = ln >> 5;
    unsigned v[8];
#pragma unroll
    for (int j = 0; j < 8; ++j) {
      const int k = perm ? (16 * s + 8 * (j >> 2) + 4 * h + (j & 3)) : (16 * s + 8 * h + j);
      v[j] = W[(size_t)k * ncols + m];
    }
    u32x4 o = {v[0] | (v[1] << 16), v[2] | (v[3] << 16), v[4] | (v[5] << 16), v[6] | (v[7] << 16)};
    *(u32x4 *)(p.ws + dst + (size_t)gg * 16) = o;
  }
  if (b < 36) {
    const u16 *W; int ncols, col; float *dst;
    if (b < 8) { W = p.w1; ncols = 512; col = 64 * b + lane; dst = (float *)(p.ws + WS_CS1); }
    else { W = p.w3; ncols = 1792; col = 64 * (b - 8) + lane; dst = (float *)(p.ws + WS_CS3); }
    float s = 0;
    for (int r = 0; r < 64; ++r) s += bf2f(W[(size_t)(64 * w + r) * ncols + col]);
    red[w][lane] = s;
    __syncthreads();
    if (w == 0) {
      float t = 0;
#pragma unroll
      for (int i = 0; i < 8; ++i) t += red[i][lane];
      dst[col] = t;
    }
  }
}

constexpr int CV_U = 0;
constexpr int CV_XT = 15360;
constexpr int CV_KT = CV_XT + 18432;
constexpr int CV_Z = CV_KT + 9216;
constexpr int CV_R = CV_Z, CV_L = CV_Z + 2 * 32 * 528;
constexpr int KC_STRIDE = 4416;
constexpr int CV_END = CV_Z + 67584;

struct cplx { float r, i; };
DI cplx cmul(cplx a, cplx b) { return cplx{a.r * b.r - a.i * b.i, a.r * b.i + a.i * b.r}; }
DI cplx zpow(float er, float tr, float pw) {
  const float mag = __builtin_amdgcn_exp2f(er * pw);
  const float ph = __builtin_amdgcn_fractf(tr * pw);
  return cplx{mag * __builtin_amdgcn_cosf(ph), mag * __builtin_amdgcn_sinf(ph)};
}

__global__ void __launch_bounds__(512, 2) k_conv(Params p) {
  __shared__ __attribute__((aligned(16))) unsigned char sm[CV_END];
  const int tid = threadIdx.x, lane = tid & 63, w = __builtin_amdgcn_readfirstlane(tid >> 6), b = blockIdx.x;
  const int pr = (b & 7) * 32 + (b >> 3), d0 = 2 * pr;
  const int ch = w >> 2, q = w & 3;

  unsigned xv[4]; f32x2 stv[4];
#pragma unroll
  for (int i = 0; i < 4; ++i) {
    xv[i] = *(const unsigned *)(p.x + (size_t)(4 * tid + i) * D_ + d0);
    stv[i] = ((const f32x2 *)(p.ws + WS_ST0))[4 * tid + i];
  }
  const int d = d0 + ch;
  const float dt = bfr(expf(bf2f(p.logdt[d])));
  float S;
  {
    const float c0r = bf2f(p.Cre[d * NS_ + lane]) * dt, c0i = bf2f(p.Cim[d * NS_ + lane]) * dt;
    const float c1r = bf2f(p.Cre[d * NS_ + 64 + lane]) * dt, c1i = bf2f(p.Cim[d * NS_ + 64 + lane]) * dt;
    float mx = fmaxf(fmaxf(fabsf(c0r), fabsf(c0i)), fmaxf(fabsf(c1r), fabsf(c1i)));
    mx = wave_max(mx);
    int e = 0;
    if (mx > 0.f && mx < 3.0e38f) (void)frexpf(mx, &e);
    e = e < -100 ? -100 : (e > 100 ? 100 : e);
    S = ldexpf(1.0f, -e);
    const int n = 64 * (q & 1) + lane;
    const cplx C = (q & 1) ? cplx{c1r * S, c1i * S} : cplx{c0r * S, c0i * S};
    const float Are = -bfr(expf(bf2f(p.logA[d * NS_ + n])));
    const float ar = Are * dt, ai = bf2f(p.Aim[d * NS_ + n]) * dt;
    const float er = ar * 1.4426950408889634f, tr = ai * 0.15915494309189535f;
    const int jh = q >> 1;
    unsigned *Rim = (unsigned *)(sm + CV_R + ch * 32 * 528);
    unsigned *Lim = (unsigned *)(sm + CV_L + ch * 32 * 528);
    cplx cur = zpow(er, tr, 128.0f * jh);
    const cplx step = zpow(er, tr, 8.0f);
#pragma unroll
    for (int jj = 0; jj < 16; ++jj) {
      Rim[(16 * jh + jj) * 132 + n] = pack_f16(cur.r, cur.i);
      cur = cmul(cur, step);
    }
    if (jh == 0) {
      const cplx z1 = zpow(er, tr, 1.0f), z256 = zpow(er, tr, 256.0f);
      cplx g = C;
#pragma unroll
      for (int r = 0; r < 8; ++r) {
        Lim[r * 132 + n] = pack_f16(g.r, -g.i);
        if ((r & 1) == 0) { cplx t = cmul(g, z256); Lim[(8 + (r >> 1)) * 132 + n] = pack_f16(t.r, -t.i); }
        g = cmul(g, z1);
      }
    } else {
      const cplx g4 = cmul(C, zpow(er, tr, 4.0f));
      const cplx z512 = zpow(er, tr, 512.0f), z768 = zpow(er, tr, 768.0f);
      cplx t;
      t = cmul(C, z512);  Lim[12 * 132 + n] = pack_f16(t.r, -t.i);
      t = cmul(g4, z512); Lim[13 * 132 + n] = pack_f16(t.r, -t.i);
      t = cmul(C, z768);  Lim[14 * 132 + n] = pack_f16(t.r, -t.i);
      t = cmul(g4, z768); Lim[15 * 132 + n] = pack_f16(t.r, -t.i);
#pragma unroll
      for (int i = 0; i < 5; ++i) {
        t = cmul(C, zpow(er, tr, 1024.0f + 256.0f * i));
        Lim[(16 + i) * 132 + n] = pack_f16(t.r, -t.i);
      }
    }
  }
  {
    const float D0 = bf2f(p.Dskip[d0]), D1 = bf2f(p.Dskip[d0 + 1]);
    unsigned u0[2], u1[2]; u32x4 xt0, xt1;
    float uu0[4], uu1[4];
#pragma unroll
    for (int i = 0; i < 4; ++i) {
      const float x0 = bflo(xv[i]), x1 = bfhi(xv[i]);
      uu0[i] = bfr(bfr(x0 - stv[i][0]) / stv[i][1]);
      uu1[i] = bfr(bfr(x1 - stv[i][0]) / stv[i][1]);
      xt0[i] = (xv[i] & 0xffffu) | (pack_bf16(0.f, uu0[i] * D0) & 0xffff0000u);
      xt1[i] = (xv[i] >> 16) | (pack_bf16(0.f, uu1[i] * D1) & 0xffff0000u);
    }
    u0[0] = pack_f16(uu0[0], uu0[1]); u0[1] = pack_f16(uu0[2], uu0[3]);
    u1[0] = pack_f16(uu1[0], uu1[1]); u1[1] = pack_f16(uu1[2], uu1[3]);
    const int l0 = 4 * tid, a = l0 >> 5, bb = l0 & 31;
    *(u32x2 *)(sm + CV_U + ((a + 32) * 40 + bb) * 2) = u32x2{u0[0], u0[1]};
    *(u32x2 *)(sm + CV_U + 7680 + ((a + 32) * 40 + bb) * 2) = u32x2{u1[0], u1[1]};
    *(u32x4 *)(sm + CV_XT + (l0 + 4 * a) * 4) = xt0;
    *(u32x4 *)(sm + CV_XT + 9216 + (l0 + 4 * a) * 4) = xt1;
    if (tid < 320) {
      const int c2 = tid / 160, o = tid % 160;
      *(u32x4 *)(sm + CV_U + c2 * 7680 + o * 16) = u32x4{0, 0, 0, 0};
    }
  }
  __syncthreads();
  {
    const unsigned char *Rb = sm + CV_R + ch * 32 * 528 + (16 * (q >> 1) + (lane & 15)) * 528 + (lane >> 4) * 16;
    const unsigned char *Lb = sm + CV_L + ch * 32 * 528 + (16 * (q & 1) + (lane & 15)) * 528 + (lane >> 4) * 16;
    f32x4 acc = {0, 0, 0, 0};
#pragma unroll
    for (int s = 0; s < 8; ++s) acc = mfma16_f16(*(const u32x4 *)(Rb + s * 64), *(const u32x4 *)(Lb + s * 64), acc);
    f32x4 o = acc * 2.0f;
    *(f32x4 *)(sm + CV_KT + ch * 4608 + ((16 * (q & 1) + (lane & 15)) * 36 + 16 * (q >> 1) + 4 * (lane >> 4)) * 4) = o;
  }
  __syncthreads();
  for (int t = tid; t < 2 * 521; t += 512) {
    const int c2 = t / 521, g = t % 521;
    const float *Kt = (const float *)(sm + CV_KT + c2 * 4608);
    float v[4];
#pragma unroll
    for (int i = 0; i < 4; ++i) {
      const int tau = 2048 - (4 * g + i);
      float val = 0.f;
      if (tau >= 0 && tau < 2048) {
        const int P = (int)bfr((float)tau);
        int col, j;
        if (P < 256) { col = P & 7; j = P >> 3; }
        else if (P < 512) { col = 8 + ((P & 7) >> 1); j = (P - 256) >> 3; }
        else if (P < 1024) { col = 12 + 2 * ((P - 512) >> 8) + ((P & 7) >> 2); j = (P & 255) >> 3; }
        else if (P < 2048) { col = 16 + ((P - 1024) >> 8); j = (P & 255) >> 3; }
        else { col = 20; j = 0; }
        val = Kt[col * 36 + j];
      }
      v[i] = val;
    }
    *(u32x2 *)(sm + CV_Z + c2 * 4 * KC_STRIDE + g * 8) = u32x2{pack_f16(v[0], v[1]), pack_f16(v[2], v[3])};
  }
  __syncthreads();
  for (int t = tid; t < 2 * 521; t += 512) {
    const int c2 = t / 521, g = t % 521;
    unsigned char *base = sm + CV_Z + c2 * 4 * KC_STRIDE;
    const u32x2 cur = *(const u32x2 *)(base + g * 8);
    u32x2 prev = {0, 0};
    if (g > 0) prev = *(const u32x2 *)(base + g * 8 - 8);
    u32x2 o1 = {__builtin_amdgcn_alignbit(cur[0], prev[1], 16), __builtin_amdgcn_alignbit(cur[1], cur[0], 16)};
    u32x2 o2 = {prev[1], cur[0]};
    u32x2 o3 = {__builtin_amdgcn_alignbit(prev[1], prev[0], 16), __builtin_amdgcn_alignbit(cur[0], prev[1], 16)};
    *(u32x2 *)(base + 1 * KC_STRIDE + g * 8) = o1;
    *(u32x2 *)(base + 2 * KC_STRIDE + g * 8) = o2;
    *(u32x2 *)(base + 3 * KC_STRIDE + g * 8) = o3;
  }
  __syncthreads();
  f32x16 acc0, acc1;
#pragma unroll
  for (int i = 0; i < 16; ++i) { acc0[i] = 0.f; acc1[i] = 0.f; }
  {
    const int m = lane & 31, h = lane >> 5, c = m & 3, mu = m >> 2;
    const unsigned char *Ab = sm + CV_Z + ch * 4 * KC_STRIDE + c * KC_STRIDE + (2048 - 4 * mu + 8 * h) * 2;
    const unsigned char *Bb = sm + CV_U + ch * 7680 + ((m + 32) * 40 + 8 * h) * 2;
#pragma unroll
    for (int dd = 0; dd < 8; ++dd) {
      const int dl = 8 * q + dd;
#pragma unroll
      for (int s = 0; s < 2; ++s) {
        const int e0 = 2 * dl - s, e1 = e0 + 64;
        const u32x2 a0l = *(const u32x2 *)(Ab - 32 * e0), a0h = *(const u32x2 *)(Ab - 32 * e0 + 8);
        const u32x2 a1l = *(const u32x2 *)(Ab - 32 * e1), a1h = *(const u32x2 *)(Ab - 32 * e1 + 8);
        const u32x4 b0 = *(const u32x4 *)(Bb + (-dl) * 80 + s * 32);
        const u32x4 b1 = *(const u32x4 *)(Bb + (32 - dl) * 80 + s * 32);
        const u32x4 a0 = {a0l[0], a0l[1], a0h[0], a0h[1]};
        const u32x4 a1 = {a1l[0], a1l[1], a1h[0], a1h[1]};
        acc0 = mfma_f16(a0, b0, acc0);
        acc1 = mfma_f16(a0, b1, acc1);
        acc1 = mfma_f16(a1, b0, acc1);
      }
    }
  }
  __syncthreads();
  {
    float *red = (float *)(sm + CV_Z);
#pragma unroll
    for (int k = 0; k < 4; ++k) {
      if (k == q) continue;
      const f32x16 &a = (k >> 1) ? acc1 : acc0;
      const int o = 8 * (k & 1);
      float *dst = red + (((ch * 4 + k) * 4 + q) * 64 + lane) * 8;
      float *dstA = red + ((((ch * 4 + k) * 4 + q) * 2 + 0) * 64 + lane) * 4;
      float *dstB = red + ((((ch * 4 + k) * 4 + q) * 2 + 1) * 64 + lane) * 4;
      (void)dst;
      *(f32x4 *)dstA = f32x4{a[o], a[o + 1], a[o + 2], a[o + 3]};
      *(f32x4 *)dstB = f32x4{a[o + 4], a[o + 5], a[o + 6], a[o + 7]};
    }
  }
  __syncthreads();
  {
    const float *red = (const float *)(sm + CV_Z);
    float y[8];
    const int o = 8 * (q & 1);
#pragma unroll
    for (int i = 0; i < 8; ++i) y[i] = (q >> 1) ? acc1[o + i] : acc0[o + i];
#pragma unroll
    for (int k = 0; k < 4; ++k) {
      if (k == q) continue;
      const f32x4 va = *(const f32x4 *)(red + ((((ch * 4 + q) * 4 + k) * 2 + 0) * 64 + lane) * 4);
      const f32x4 vb = *(const f32x4 *)(red + ((((ch * 4 + q) * 4 + k) * 2 + 1) * 64 + lane) * 4);
#pragma unroll
      for (int i = 0; i < 4; ++i) { y[i] += va[i]; y[4 + i] += vb[i]; }
    }
    const float invS = 1.0f / S;
    const int n = lane & 31, h = lane >> 5, t = q >> 1, rh = q & 1;
    const int a = 32 * t + n;
    u16 *Hout = (u16 *)(sm + CV_KT) + ch * 2048;
#pragma unroll
    for (int g = 0; g < 2; ++g) {
      const int bb = 16 * rh + 8 * g + 4 * h;
      const int l0 = 32 * a + bb;
      const u32x4 xt = *(const u32x4 *)(sm + CV_XT + ch * 9216 + (l0 + 4 * a) * 4);
      float hv[4];
#pragma unroll
      for (int i = 0; i < 4; ++i) {
        const float yy = bfr(y[4 * g + i] * invS);
        hv[i] = bflo(xt[i]) + bfr(yy + bfhi(xt[i]));
      }
      *(u32x2 *)(Hout + l0) = u32x2{pack_bf16(hv[0], hv[1]), pack_bf16(hv[2], hv[3])};
    }
  }
  __syncthreads();
  {
    const u16 *Hout = (const u16 *)(sm + CV_KT);
    const u32x2 a = *(const u32x2 *)(Hout + 4 * tid), c = *(const u32x2 *)(Hout + 2048 + 4 * tid);
    u32x4 o;
    o[0] = (a[0] & 0xffffu) | (c[0] << 16);
    o[1] = (a[0] >> 16) | (c[0] & 0xffff0000u);
    o[2] = (a[1] & 0xffffu) | (c[1] << 16);
    o[3] = (a[1] >> 16) | (c[1] & 0xffff0000u);
    *(u32x4 *)(p.ws + WS_HP + ((size_t)pr * 2048 + 4 * tid) * 4) = o;
  }
}

DI void team_map(int b, int &T, int &j) { T = 4 * (b & 7) + ((b >> 3) >> 3); j = (b >> 3) & 7; }

__global__ void __launch_bounds__(512, 2) k_g1(Params p) {
  __shared__ __attribute__((aligned(16))) unsigned char sm[32768 + 8192 + 16384];
  const int tid = threadIdx.x, lane = tid & 63, w = __builtin_amdgcn_readfirstlane(tid >> 6);
  int T, j; team_map(blockIdx.x, T, j);
  const int tt = 2 * T + (j >> 2), c = j & 3;
  const int t = lane & 31, h = lane >> 5;
  const int sh = (c == 0) ? 1 : (c == 1) ? -1 : (c == 2) ? 2 : -2;
  const int tok = 32 * tt - sh + t;
  const bool valid = tok >= 0 && tok < L_;
  const int tokc = valid ? tok : 0;
  const int mi = w & 3, kh = w >> 2;
  u32x4 A[16];
  {
    const u32x4 *wp = (const u32x4 *)(p.ws + WS_W1T) + ((size_t)((4 * c + mi) * 32 + 16 * kh) * 64 + lane);
#pragma unroll
    for (int s = 0; s < 16; ++s) A[s] = wp[s * 64];
  }
  {
    const unsigned *hp = (const unsigned *)(p.ws + WS_HP);
    float s1 = 0.f, s2 = 0.f;
#pragma unroll
    for (int ks = 0; ks < 4; ++ks) {
      const int s = 4 * w + ks;
      u32x4 v;
#pragma unroll
      for (int i = 0; i < 4; ++i) v[i] = hp[(size_t)(8 * s + 4 * h + i) * 2048 + tokc];
#pragma unroll
      for (int i = 0; i < 4; ++i) { s1 = dot2bf(v[i], 0x3f803f80u, s1); s2 = dot2bf(v[i], v[i], s2); }
      *(u32x4 *)(sm + (s * 64 + lane) * 16) = v;
    }
    *(f32x2 *)(sm + 32768 + (w * 64 + lane) * 8) = f32x2{s1, s2};
  }
  __syncthreads();
  f32x16 acc;
#pragma unroll
  for (int i = 0; i < 16; ++i) acc[i] = 0.f;
#pragma unroll
  for (int s = 0; s < 16; ++s) acc = mfma_bf16(A[s], *(const u32x4 *)(sm + ((16 * kh + s) * 64 + lane) * 16), acc);
  float s1 = 0.f, s2 = 0.f;
#pragma unroll
  for (int i = 0; i < 16; ++i) { const f32x2 v = *(const f32x2 *)(sm + 32768 + (i * 32 + t) * 8); s1 += v[0]; s2 += v[1]; }
  const float mean = s1 * (1.0f / D_);
  const float var = fmaxf(s2 * (1.0f / D_) - mean * mean, 0.f);
  const float m1 = bfr(mean), sd1 = bfr(sqrtf(bfr(bfr(var) + 1e-5f)));
  const float mean2 = (mean - m1) / sd1, var2 = var / (sd1 * sd1);
  const float m2 = bfr(mean2), sd2 = bfr(sqrtf(bfr(bfr(var2) + 1e-5f)));
  const float alpha = 1.0f / (sd1 * sd2), beta = -(m1 * alpha + m2 / sd2);
  if (w == 0 && h == 0) ((f32x4 *)(p.ws + WS_ST1))[(tt * 4 + c) * 32 + t] = f32x4{m1, sd1, 0.f, 0.f};
  {
    float *ex = (float *)(sm + 32768 + 8192);
    const int o = 8 * (1 - kh);
    *(f32x4 *)(ex + ((w * 2 + 0) * 64 + lane) * 4) = kh ? f32x4{acc[0], acc[1], acc[2], acc[3]} : f32x4{acc[8], acc[9], acc[10], acc[11]};
    *(f32x4 *)(ex + ((w * 2 + 1) * 64 + lane) * 4) = kh ? f32x4{acc[4], acc[5], acc[6], acc[7]} : f32x4{acc[12], acc[13], acc[14], acc[15]};
    (void)o;
  }
  __syncthreads();
  {
    const float *ex = (const float *)(sm + 32768 + 8192);
    const int pw = w ^ 4;
    const f32x4 va = *(const f32x4 *)(ex + ((pw * 2 + 0) * 64 + lane) * 4);
    const f32x4 vb = *(const f32x4 *)(ex + ((pw * 2 + 1) * 64 + lane) * 4);
    float v[8];
#pragma unroll
    for (int i = 0; i < 4; ++i) { v[i] = (kh ? acc[8 + i] : acc[i]) + va[i]; v[4 + i] = (kh ? acc[12 + i] : acc[4 + i]) + vb[i]; }
    const float *cs = (const float *)(p.ws + WS_CS1) + 128 * c + 32 * mi + 16 * kh + 4 * h;
    const f32x4 ca = *(const f32x4 *)cs, cb = *(const f32x4 *)(cs + 8);
    float o[8];
#pragma unroll
    for (int i = 0; i < 4; ++i) {
      o[i] = gelu_tanh(bfr(alpha * v[i] + beta * ca[i]));
      o[4 + i] = gelu_tanh(bfr(alpha * v[4 + i] + beta * cb[i]));
    }
    u32x4 pk = {pack_bf16(o[0], o[1]), pack_bf16(o[2], o[3]), pack_bf16(o[4], o[5]), pack_bf16(o[6], o[7])};
    if (!valid) pk = u32x4{0, 0, 0, 0};
    const int kstep = 8 * c + 2 * mi + kh;
    *(u32x4 *)(p.ws + WS_H1 + ((size_t)(2 * kstep + h) * 2048 + 32 * tt + t) * 16) = pk;
  }
}

__global__ void __launch_bounds__(512, 2) k_g2(Params p) {
  __shared__ __attribute__((aligned(16))) unsigned char sm[32768 + 16384];
  const int tid = threadIdx.x, lane = tid & 63, w = __builtin_amdgcn_readfirstlane(tid >> 6);
  int T, j; team_map(blockIdx.x, T, j);
  const int tt = 2 * T + (j >> 2), c = j & 3;
  const int t = lane & 31, h = lane >> 5, tok = 32 * tt + t;
  const int mi = w & 3, kh = w >> 2;
  u32x4 A[16];
  {
    const u32x4 *wp = (const u32x4 *)(p.ws + WS_W2T) + ((size_t)((4 * c + mi) * 32 + 16 * kh) * 64 + lane);
#pragma unroll
    for (int s = 0; s < 16; ++s) A[s] = wp[s * 64];
  }
#pragma unroll
  for (int ks = 0; ks < 4; ++ks) {
    const int s = 4 * w + ks;
    const u32x4 v = *(const u32x4 *)(p.ws + WS_H1 + ((size_t)(2 * s + h) * 2048 + tok) * 16);
    *(u32x4 *)(sm + (s * 64 + lane) * 16) = v;
  }
  const int f0 = 128 * c + 32 * mi + 16 * kh + 4 * h;
  unsigned hv[4];
  {
    const unsigned *hp = (const unsigned *)(p.ws + WS_HP);
    hv[0] = hp[(size_t)(f0 / 2) * 2048 + tok];
    hv[1] = hp[(size_t)(f0 / 2 + 1) * 2048 + tok];
    hv[2] = hp[(size_t)(f0 / 2 + 4) * 2048 + tok];
    hv[3] = hp[(size_t)(f0 / 2 + 5) * 2048 + tok];
  }
  const f32x4 st = (t < 31) ? ((const f32x4 *)(p.ws + WS_ST1))[(tt * 4 + 0) * 32 + t + 1] : ((const f32x4 *)(p.ws + WS_ST1))[(tt * 4 + 1) * 32 + 30];
  __syncthreads();
  f32x16 acc;
#pragma unroll
  for (int i = 0; i < 16; ++i) acc[i] = 0.f;
#pragma unroll
  for (int s = 0; s < 16; ++s) acc = mfma_bf16(A[s], *(const u32x4 *)(sm + ((16 * kh + s) * 64 + lane) * 16), acc);
  {
    float *ex = (float *)(sm + 32768);
    *(f32x4 *)(ex + ((w * 2 + 0) * 64 + lane) * 4) = kh ? f32x4{acc[0], acc[1], acc[2], acc[3]} : f32x4{acc[8], acc[9], acc[10], acc[11]};
    *(f32x4 *)(ex + ((w * 2 + 1) * 64 + lane) * 4) = kh ? f32x4{acc[4], acc[5], acc[6], acc[7]} : f32x4{acc[12], acc[13], acc[14], acc[15]};
  }
  __syncthreads();
  {
    const float *ex = (const float *)(sm + 32768);
    const int pw = w ^ 4;
    const f32x4 va = *(const f32x4 *)(ex + ((pw * 2 + 0) * 64 + lane) * 4);
    const f32x4 vb = *(const f32x4 *)(ex + ((pw * 2 + 1) * 64 + lane) * 4);
    float v[8];
#pragma unroll
    for (int i = 0; i < 4; ++i) { v[i] = (kh ? acc[8 + i] : acc[i]) + va[i]; v[4 + i] = (kh ? acc[12 + i] : acc[4 + i]) + vb[i]; }
    float o[8];
    const float m1 = st[0], sd1 = st[1];
#pragma unroll
    for (int i = 0; i < 4; ++i) {
      const float h0 = bflo(hv[i]), h1 = bfhi(hv[i]);
      o[2 * i] = bfr(bfr(h0 - m1) / sd1) + bfr(v[2 * i]);
      o[2 * i + 1] = bfr(bfr(h1 - m1) / sd1) + bfr(v[2 * i + 1]);
    }
    u32x4 pk = {pack_bf16(o[0], o[1]), pack_bf16(o[2], o[3]), pack_bf16(o[4], o[5]), pack_bf16(o[6], o[7])};
    const int kstep = 8 * c + 2 * mi + kh;
    *(u32x4 *)(p.ws + WS_X1 + ((size_t)(2 * kstep + h) * 2048 + tok) * 16) = pk;
  }
}

__global__ void __launch_bounds__(512, 2) k_g34(Params p) {
  __shared__ __attribute__((aligned(16))) unsigned char sm[65536 + 28672 + 8192];
  const int tid = threadIdx.x, lane = tid & 63, w = __builtin_amdgcn_readfirstlane(tid >> 6);
  int T, j; team_map(blockIdx.x, T, j);
  const int t = lane & 31, h = lane >> 5;
  u32x4 A[32];
  const int w7 = w < 7 ? w : 6;
  {
    const u32x4 *wp = (const u32x4 *)(p.ws + WS_W3T) + ((size_t)((7 * j + w7) * 32) * 64 + lane);
#pragma unroll
    for (int s = 0; s < 32; ++s) A[s] = wp[s * 64];
  }
  {
    const int tl = w >> 2;
    float s1 = 0.f, s2 = 0.f;
#pragma unroll
    for (int ks = 0; ks < 8; ++ks) {
      const int s = 8 * (w & 3) + ks;
      const u32x4 v = *(const u32x4 *)(p.ws + WS_X1 + ((size_t)(2 * s + h) * 2048 + 64 * T + 32 * tl + t) * 16);
#pragma unroll
      for (int i = 0; i < 4; ++i) { s1 = dot2bf(v[i], 0x3f803f80u, s1); s2 = dot2bf(v[i], v[i], s2); }
      *(u32x4 *)(sm + ((tl * 32 + s) * 64 + lane) * 16) = v;
    }
    *(f32x2 *)(sm + 65536 + 28672 + (w * 64 + lane) * 8) = f32x2{s1, s2};
  }
  __syncthreads();
  f32x16 acc0, acc1;
#pragma unroll
  for (int i = 0; i < 16; ++i) { acc0[i] = 0.f; acc1[i] = 0.f; }
  if (w < 7) {
#pragma unroll
    for (int s = 0; s < 32; ++s) {
      acc0 = mfma_bf16(A[s], *(const u32x4 *)(sm + ((0 * 32 + s) * 64 + lane) * 16), acc0);
      acc1 = mfma_bf16(A[s], *(const u32x4 *)(sm + ((1 * 32 + s) * 64 + lane) * 16), acc1);
    }
  }
  u32x4 A4[28];
  {
    const u32x4 *wp = (const u32x4 *)(p.ws + WS_W4T) + ((size_t)((2 * w) * 112 + 14 * j) * 64 + lane);
#pragma unroll
    for (int s = 0; s < 14; ++s) { A4[s] = wp[s * 64]; A4[14 + s] = wp[(112 + s) * 64]; }
  }
  if (w < 7) {
    float r3[2], mr3[2];
#pragma unroll
    for (int tl = 0; tl < 2; ++tl) {
      float s1 = 0.f, s2 = 0.f;
#pragma unroll
      for (int i = 0; i < 8; ++i) {
        const f32x2 v = *(const f32x2 *)(sm + 65536 + 28672 + ((tl * 4 + (i >> 1)) * 64 + (i & 1) * 32 + t) * 8);
        s1 += v[0]; s2 += v[1];
      }
      const float mean = s1 * (1.0f / D_);
      const float var = fmaxf(s2 * (1.0f / D_) - mean * mean, 0.f);
      const float m3 = bfr(mean), sd3 = bfr(sqrtf(bfr(bfr(var) + 1e-5f)));
      r3[tl] = 1.0f / sd3; mr3[tl] = -m3 * r3[tl];
    }
    const float *cs = (const float *)(p.ws + WS_CS3) + 32 * (7 * j + w7) + 4 * h;
    f32x4 cv[4];
#pragma unroll
    for (int g = 0; g < 4; ++g) cv[g] = *(const f32x4 *)(cs + 8 * g);
#pragma unroll
    for (int tl = 0; tl < 2; ++tl) {
      const f32x16 &a = tl ? acc1 : acc0;
      float o[16];
#pragma unroll
      for (int i = 0; i < 16; ++i) o[i] = gelu_tanh(bfr(r3[tl] * a[i] + mr3[tl] * cv[i >> 2][i & 3]));
#pragma unroll
      for (int s = 0; s < 2; ++s) {
        u32x4 pk = {pack_bf16(o[8 * s], o[8 * s + 1]), pack_bf16(o[8 * s + 2], o[8 * s + 3]), pack_bf16(o[8 * s + 4], o[8 * s + 5]), pack_bf16(o[8 * s + 6], o[8 * s + 7])};
        *(u32x4 *)(sm + 65536 + ((tl * 14 + 2 * w + s) * 64 + lane) * 16) = pk;
      }
    }
  }
  __syncthreads();
  f32x16 o00, o01, o10, o11;
#pragma unroll
  for (int i = 0; i < 16; ++i) { o00[i] = 0.f; o01[i] = 0.f; o10[i] = 0.f; o11[i] = 0.f; }
#pragma unroll
  for (int s = 0; s < 14; ++s) {
    const u32x4 b0 = *(const u32x4 *)(sm + 65536 + ((0 * 14 + s) * 64 + lane) * 16);
    const u32x4 b1 = *(const u32x4 *)(sm + 65536 + ((1 * 14 + s) * 64 + lane) * 16);
    o00 = mfma_bf16(A4[s], b0, o00);
    o01 = mfma_bf16(A4[s], b1, o01);
    o10 = mfma_bf16(A4[14 + s], b0, o10);
    o11 = mfma_bf16(A4[14 + s], b1, o11);
  }
  {
    unsigned char *slab = p.ws + WS_SLAB + (size_t)j * 2 * 1024 * 1024;
#pragma unroll
    for (int mt = 0; mt < 2; ++mt)
#pragma unroll
      for (int tl = 0; tl < 2; ++tl) {
        const f32x16 &a = mt ? (tl ? o11 : o10) : (tl ? o01 : o00);
#pragma unroll
        for (int s = 0; s < 2; ++s) {
          u32x4 pk = {pack_bf16(a[8 * s], a[8 * s + 1]), pack_bf16(a[8 * s + 2], a[8 * s + 3]), pack_bf16(a[8 * s + 4], a[8 * s + 5]), pack_bf16(a[8 * s + 6], a[8 * s + 7])};
          const int kstep = 2 * (2 * w + mt) + s;
          *(u32x4 *)(slab + ((size_t)(2 * kstep + h) * 2048 + 64 * T + 32 * tl + t) * 16) = pk;
        }
      }
  }
}

__global__ void __launch_bounds__(512, 2) k_fin(Params p) {
  const int tid = threadIdx.x;
  int T, j; team_map(blockIdx.x, T, j);
  const int h = tid & 1, ks = (tid >> 1) & 3, tk = tid >> 3;
  const int tok = 64 * T + tk, kstep = 4 * j + ks, chunk = 2 * kstep + h;
  float acc[8];
#pragma unroll
  for (int i = 0; i < 8; ++i) acc[i] = 0.f;
  u32x4 sl[8];
#pragma unroll
  for (int q = 0; q < 8; ++q) sl[q] = *(const u32x4 *)(p.ws + WS_SLAB + (size_t)q * 2 * 1024 * 1024 + ((size_t)chunk * 2048 + tok) * 16);
  const u32x4 x1 = *(const u32x4 *)(p.ws + WS_X1 + ((size_t)chunk * 2048 + tok) * 16);
  const int f0 = 16 * kstep + 4 * h;
  const unsigned *hp = (const unsigned *)(p.ws + WS_HP);
  unsigned hv[4];
  hv[0] = hp[(size_t)(f0 / 2) * 2048 + tok];
  hv[1] = hp[(size_t)(f0 / 2 + 1) * 2048 + tok];
  hv[2] = hp[(size_t)(f0 / 2 + 4) * 2048 + tok];
  hv[3] = hp[(size_t)(f0 / 2 + 5) * 2048 + tok];
#pragma unroll
  for (int q = 0; q < 8; ++q)
#pragma unroll
    for (int i = 0; i < 4; ++i) { acc[2 * i] += bflo(sl[q][i]); acc[2 * i + 1] += bfhi(sl[q][i]); }
  float o[8];
#pragma unroll
  for (int i = 0; i < 4; ++i) {
    o[2 * i] = bflo(hv[i]) + bfr(bflo(x1[i]) + bfr(acc[2 * i]));
    o[2 * i + 1] = bfhi(hv[i]) + bfr(bfhi(x1[i]) + bfr(acc[2 * i + 1]));
  }
  u16 *dst = p.out + (size_t)tok * D_ + f0;
  *(u32x2 *)dst = u32x2{pack_bf16(o[0], o[1]), pack_bf16(o[2], o[3])};
  *(u32x2 *)(dst + 8) = u32x2{pack_bf16(o[4], o[5]), pack_bf16(o[6], o[7])};
  asm volatile("text_end_marker:");
}

extern "C" void kernel_launch(void *const *d_in, const int *in_sizes, int n_in, void *d_out, int out_size, void *d_ws,
                              size_t ws_size, hipStream_t stream) {
  Params p;
  p.x = (const u16 *)d_in[0];
  p.logA = (const u16 *)d_in[1];
  p.Aim = (const u16 *)d_in[2];
  p.Cre = (const u16 *)d_in[3];
  p.Cim = (const u16 *)d_in[4];
  p.logdt = (const u16 *)d_in[5];
  p.Dskip = (const u16 *)d_in[6];
  p.w1 = (const u16 *)d_in[8];
  p.w2 = (const u16 *)d_in[9];
  p.w3 = (const u16 *)d_in[10];
  p.w4 = (const u16 *)d_in[11];
  p.out = (u16 *)d_out;
  p.ws = (unsigned char *)d_ws;
  k_prep<<<256, 512, 0, stream>>>(p);
  k_conv<<<256, 512, 0, stream>>>(p);
  k_g1<<<256, 512, 0, stream>>>(p);
  k_g2<<<256, 512, 0, stream>>>(p);
  k_g34<<<256, 512, 0, stream>>>(p);
  k_fin<<<256, 512, 0, stream>>>(p);
}
```

```cpp
#include <hip/hip_runtime.h>
#include <hip/hip_bf16.h>
#include <stdint.h>

#define DI __device__ __forceinline__
#define GAS __attribute__((address_space(1)))
#define LAS __attribute__((address_space(3)))
typedef __attribute__((ext_vector_type(8))) short bf16x8;
typedef __attribute__((ext_vector_type(8))) _Float16 f16x8;
typedef __attribute__((ext_vector_type(2))) _Float16 f16x2;
typedef __attribute__((ext_vector_type(2))) __bf16 bf16x2;
typedef __attribute__((ext_vector_type(16))) float f32x16;
typedef __attribute__((ext_vector_type(4))) float f32x4;
typedef __attribute__((ext_vector_type(2))) float f32x2;
typedef __attribute__((ext_vector_type(4))) unsigned u32x4;
typedef __attribute__((ext_vector_type(2))) unsigned u32x2;
typedef unsigned short u16;

constexpr int L_ = 2048, D_ = 512, NS_ = 128;

constexpr unsigned WS_EPOCH = 0;
constexpr unsigned WS_ABORT = 256;
constexpr unsigned WS_FW = 4096;
constexpr unsigned WS_FZ = WS_FW + 8192;
constexpr unsigned WS_F1 = WS_FZ + 8192;
constexpr unsigned WS_F2 = WS_F1 + 8192;
constexpr unsigned WS_F3 = WS_F2 + 8192;
constexpr unsigned WS_ST1 = 65536;
constexpr unsigned WS_W1T = 131072;
constexpr unsigned WS_W2T = WS_W1T + 512 * 1024;
constexpr unsigned WS_W3T = WS_W2T + 512 * 1024;
constexpr unsigned WS_W4T = WS_W3T + 56 * 32 * 1024;
constexpr unsigned WS_UG = WS_W4T + 16 * 112 * 1024;
constexpr unsigned WS_HP = WS_UG + 8 * 1024 * 1024;
constexpr unsigned WS_H1 = WS_HP + 2 * 1024 * 1024;
constexpr unsigned WS_X1 = WS_H1 + 2 * 1024 * 1024;
constexpr unsigned WS_SLAB = WS_X1 + 2 * 1024 * 1024;
constexpr unsigned WS_END = WS_SLAB + 16 * 1024 * 1024;
constexpr int AUX_SC1 = 16;

#define STAMP(i)

DI float asf(unsigned u) { return __builtin_bit_cast(float, u); }
DI unsigned asu(float f) { return __builtin_bit_cast(unsigned, f); }
DI float bf2f(unsigned b) { return asf(b << 16); }
DI float bflo(unsigned w) { return asf(w << 16); }
DI float bfhi(unsigned w) { return asf(w & 0xffff0000u); }
DI float bfr(float x) { return (float)(__bf16)x; }
DI unsigned pack_bf16(float a, float b) {
  f32x2 v = {a, b};
  bf16x2 r = __builtin_convertvector(v, bf16x2);
  return __builtin_bit_cast(unsigned, r);
}
DI unsigned pack_f16(float a, float b) {
  f32x2 v = {a, b};
  f16x2 r = __builtin_convertvector(v, f16x2);
  return __builtin_bit_cast(unsigned, r);
}
DI float rcp(float x) { return __builtin_amdgcn_rcpf(x); }
DI float gelu_tanh(float x) {
  const float c0 = -2.0f * 0.7978845608028654f * 1.4426950408889634f;
  const float c1 = c0 * 0.044715f;
  float t = x * __builtin_fmaf(x * x, c1, c0);
  float e = __builtin_amdgcn_exp2f(t);
  return x * rcp(1.0f + e);
}
DI f32x16 mfma_bf16(u32x4 a, u32x4 b, f32x16 c) {
  return __builtin_amdgcn_mfma_f32_32x32x16_bf16(__builtin_bit_cast(bf16x8, a), __builtin_bit_cast(bf16x8, b), c, 0, 0, 0);
}
DI f32x16 mfma_f16(u32x4 a, u32x4 b, f32x16 c) {
  return __builtin_amdgcn_mfma_f32_32x32x16_f16(__builtin_bit_cast(f16x8, a), __builtin_bit_cast(f16x8, b), c, 0, 0, 0);
}
DI f32x4 mfma16_f16(u32x4 a, u32x4 b, f32x4 c) {
  return __builtin_amdgcn_mfma_f32_16x16x32_f16(__builtin_bit_cast(f16x8, a), __builtin_bit_cast(f16x8, b), c, 0, 0, 0);
}
template <int CTRL, int RM> DI float dpp_add(float v) {
  return v + asf((unsigned)__builtin_amdgcn_update_dpp(0, (int)asu(v), CTRL, RM, 0xf, true));
}
template <int CTRL, int RM> DI float dpp_max(float v) {
  return fmaxf(v, asf((unsigned)__builtin_amdgcn_update_dpp(0, (int)asu(v), CTRL, RM, 0xf, true)));
}
DI float wave_sum(float v) {
  v = dpp_add<0x111, 0xf>(v); v = dpp_add<0x112, 0xf>(v); v = dpp_add<0x114, 0xf>(v); v = dpp_add<0x118, 0xf>(v);
  v = dpp_add<0x142, 0xa>(v); v = dpp_add<0x143, 0xc>(v);
  return asf((unsigned)__builtin_amdgcn_readlane((int)asu(v), 63));
}
DI float wave_max(float v) {
  v = dpp_max<0x111, 0xf>(v); v = dpp_max<0x112, 0xf>(v); v = dpp_max<0x114, 0xf>(v); v = dpp_max<0x118, 0xf>(v);
  v = dpp_max<0x142, 0xa>(v); v = dpp_max<0x143, 0xc>(v);
  return asf((unsigned)__builtin_amdgcn_readlane((int)asu(v), 63));
}
DI float half_sum(float v) {
  const auto r = __builtin_amdgcn_permlane32_swap(asu(v), asu(v), false, false);
  return asf(r[0]) + asf(r[1]);
}
DI float dot2bf(unsigned a, unsigned b, float c) {
  return __builtin_amdgcn_fdot2_f32_bf16(__builtin_bit_cast(bf16x2, a), __builtin_bit_cast(bf16x2, b), c, false);
}
#define DRAIN() asm volatile("s_waitcnt vmcnt(0)" ::: "memory")
#define MEMBAR() asm volatile("" ::: "memory")
#define SCHED0() __builtin_amdgcn_sched_barrier(0)
#define SGB(mask, n) __builtin_amdgcn_sched_group_barrier(mask, n, 0)
constexpr int M_MFMA = 0x8, M_VALU = 0x2, M_VMEM_RD = 0x20, M_VMEM_WR = 0x40, M_DS_RD = 0x100, M_DS_WR = 0x200, M_TRANS = 0x400;

struct Params {
  const u16 *x, *logA, *Aim, *Cre, *Cim, *logdt, *Dskip, *w1, *w2, *w3, *w4;
  u16 *out;
  unsigned char *ws;
};

typedef __amdgpu_buffer_rsrc_t rsrc_t;
DI u32x4 ld128s(rsrc_t r, unsigned off) { return __builtin_amdgcn_raw_buffer_load_b128(r, off, 0, AUX_SC1); }
DI unsigned ld32s(rsrc_t r, unsigned off) { return __builtin_amdgcn_raw_buffer_load_b32(r, off, 0, AUX_SC1); }
DI u32x2 ld64s(rsrc_t r, unsigned off) { return __builtin_amdgcn_raw_buffer_load_b64(r, off, 0, AUX_SC1); }
DI void st128s(rsrc_t r, unsigned off, u32x4 v) { __builtin_amdgcn_raw_buffer_store_b128(v, r, off, 0, AUX_SC1); }
DI void st64s(rsrc_t r, unsigned off, u32x2 v) { __builtin_amdgcn_raw_buffer_store_b64(v, r, off, 0, AUX_SC1); }
DI void st32s(rsrc_t r, unsigned off, unsigned v) { __builtin_amdgcn_raw_buffer_store_b32(v, r, off, 0, AUX_SC1); }

constexpr unsigned SPIN_CAP = 1u << 18;
DI bool spin_fail(unsigned &spins, rsrc_t r, unsigned tag) {
  ++spins;
  if (spins > SPIN_CAP) { st32s(r, WS_ABORT, tag); return true; }
  if ((spins & 255u) == 0u) return __builtin_amdgcn_readfirstlane(ld32s(r, WS_ABORT)) == tag;
  return false;
}
DI void wait_flags256(rsrc_t r, unsigned off, unsigned tag, int lane) {
  for (unsigned spins = 0;;) {
    MEMBAR();
    const u32x4 v = ld128s(r, off + lane * 16);
    const bool ok = v[0] == tag && v[1] == tag && v[2] == tag && v[3] == tag;
    if (__all(ok)) break;
    if (spin_fail(spins, r, tag)) break;
  }
  MEMBAR();
}
DI void wait_flags8(rsrc_t r, unsigned off, unsigned tag, int lane, int lo, int n) {
  const bool mine = lane >= lo && lane < lo + n;
  const unsigned o = off + (mine ? lane : lo) * 4;
  for (unsigned spins = 0;;) {
    MEMBAR();
    const unsigned v = ld32s(r, o);
    if (__all(v == tag)) break;
    if (spin_fail(spins, r, tag)) break;
  }
  MEMBAR();
}

struct cplx { float r, i; };
DI cplx cmul(cplx a, cplx b) { return cplx{__builtin_fmaf(a.r, b.r, -(a.i * b.i)), __builtin_fmaf(a.r, b.i, a.i * b.r)}; }
DI cplx zpow(float er, float tr, float pw) {
  const float mag = __builtin_amdgcn_exp2f(er * pw);
  const float ph = __builtin_amdgcn_fractf(tr * pw);
  return cplx{mag * __builtin_amdgcn_cosf(ph), mag * __builtin_amdgcn_sinf(ph)};
}
DI float fast_exp(float x) { return __builtin_amdgcn_exp2f(x * 1.4426950408889634f); }

constexpr int CV_U = 0;
constexpr int CV_X = 15360;
constexpr int CV_KT = CV_X + 9216;
constexpr int CV_Z = CV_KT + 8192;
constexpr int CV_R = CV_Z, CV_L = CV_Z + 2 * 32 * 528;
constexpr int KC_STRIDE = 4416;
constexpr int CV_KC = CV_Z + 67584;
constexpr int CV_T = CV_KC + 35328;
constexpr int G_B = 0;
constexpr int G_H = 65536;
constexpr int G_ST = G_H + 28672;
constexpr int G_EX = G_ST + 4096;
constexpr int SM_BYTES = CV_T + 16640;
static_assert(G_EX + 16384 <= SM_BYTES, "lds");

DI void row_stats(float s1, float s2, float &m, float &sd, float &mean, float &var) {
  mean = s1 * (1.0f / D_);
  var = fmaxf(__builtin_fmaf(s2, 1.0f / D_, -mean * mean), 0.f);
  m = bfr(mean);
  sd = bfr(__builtin_amdgcn_sqrtf(bfr(bfr(var) + 1e-5f)));
}

__global__ void __launch_bounds__(512, 2) k_all(Params p) {
  __shared__ __attribute__((aligned(16))) unsigned char sm[SM_BYTES];
  STAMP(0);
  const int tid = threadIdx.x, lane = tid & 63, w = __builtin_amdgcn_readfirstlane(tid >> 6), b = blockIdx.x;
  unsigned long long pw1 = (unsigned long long)p.w1, pw2 = (unsigned long long)p.w2, pw3 = (unsigned long long)p.w3, pw4 = (unsigned long long)p.w4;
  asm volatile("" : "+s"(pw1), "+s"(pw2), "+s"(pw3), "+s"(pw4) : "s"(p.x), "s"(p.logA), "s"(p.Aim), "s"(p.Cre), "s"(p.Cim), "s"(p.logdt), "s"(p.Dskip), "s"(p.out), "s"(p.ws));
  const rsrc_t ws = __builtin_amdgcn_make_buffer_rsrc((void *)p.ws, 0, (int)WS_END, 0x00020000);
  STAMP(27);
  const u32x4 xrow = *(const GAS u32x4 *)(p.x + (size_t)(8 * b + w) * D_ + 8 * lane);
  unsigned E = ld32s(ws, WS_EPOCH);
  const int pr = (b & 7) * 32 + (b >> 3), d0 = 2 * pr;
  const int ch = w >> 2, q = w & 3;
  const int d = d0 + ch;
  const int nn = 64 * (q & 1) + lane;
  const unsigned p_logdt = ((const GAS u16 *)p.logdt)[d];
  const unsigned p_c0r = ((const GAS u16 *)p.Cre)[d * NS_ + lane], p_c0i = ((const GAS u16 *)p.Cim)[d * NS_ + lane];
  const unsigned p_c1r = ((const GAS u16 *)p.Cre)[d * NS_ + 64 + lane], p_c1i = ((const GAS u16 *)p.Cim)[d * NS_ + 64 + lane];
  const unsigned p_logA = ((const GAS u16 *)p.logA)[d * NS_ + nn], p_aim = ((const GAS u16 *)p.Aim)[d * NS_ + nn];
  const unsigned p_dsk = ((const GAS u16 *)p.Dskip)[d];
  SCHED0();
  unsigned wv[8], wv2[8];
  unsigned wdst = WS_END, wdst2 = WS_END, wstr = 0, wstr2 = 0;
  auto wissue = [&](int df, unsigned (&v)[8], unsigned &dst, unsigned &dst2) {
    const bool m1 = df < 256, m2 = df < 512, m3 = df < 1408;
    const unsigned long long W = m1 ? pw1 : m2 ? pw2 : m3 ? pw3 : pw4;
    const int ncols = m3 && !m2 ? 1792 : 512;
    const int rel = m2 ? (df & 255) : m3 ? df - 512 : df - 1408;
    const int mtp = m3 ? rel >> 5 : rel / 112, s = m3 ? rel & 31 : rel % 112, nks = m3 ? 32 : 112;
    const unsigned base = m1 ? WS_W1T : m2 ? WS_W2T : m3 ? WS_W3T : WS_W4T;
    const int c2 = lane & 31, h = lane >> 5;
    const int kh0 = m1 ? 8 * h : 4 * h, kj = m1 ? 4 : 8;
    const GAS u16 *src = (const GAS u16 *)W + (size_t)(16 * s + kh0) * ncols + 64 * mtp + 2 * c2;
#pragma unroll
    for (int j = 0; j < 8; ++j) v[j] = *(const GAS unsigned *)(src + (size_t)(kj * (j >> 2) + (j & 3)) * ncols);
    const int m = 2 * (c2 & 15) + (c2 >> 4);
    dst = base + (unsigned)(((2 * mtp * nks + s) * 64 + m + 32 * h) * 16);
    dst2 = (unsigned)nks * 1024u;
  };
  auto wstore = [&](const unsigned (&v)[8], unsigned dst, unsigned dst2) {
    u32x4 fa, fb;
#pragma unroll
    for (int i = 0; i < 4; ++i) {
      const unsigned lo = __builtin_amdgcn_perm(v[2 * i + 1], v[2 * i], 0x05040100u), hi = __builtin_amdgcn_perm(v[2 * i + 1], v[2 * i], 0x07060302u);
      const auto r = __builtin_amdgcn_permlane16_swap(lo, hi, false, false);
      fa[i] = r[0]; fb[i] = r[1];
    }
    st128s(ws, dst, fa);
    st128s(ws, dst + dst2, fb);
  };
  if (w < 2) wissue(2 * b + w, wv, wdst, wstr);
  SCHED0();
  STAMP(1);
  unsigned kix[4];
  {
    unsigned t7[8];
#pragma unroll
    for (int i = 0; i < 8; i += 2) {
      const int ta = 2047 - 4 * tid - i, tb = ta - 1;
      const unsigned bits = pack_bf16((float)ta, (float)tb);
      const unsigned ia = ta < 128 ? (unsigned)ta : (bits & 0xffffu) - 17024u, ib = tb < 128 ? (unsigned)tb : (bits >> 16) - 17024u;
      t7[i] = (ta >= 0 ? ia : 641u) * 4u;
      t7[i + 1] = (tb >= 0 ? ib : 641u) * 4u;
    }
#pragma unroll
    for (int i = 0; i < 4; ++i) { kix[i] = t7[2 * i] | (t7[2 * i + 1] << 16); asm volatile("" : "+v"(kix[i])); }
  }
  unsigned ktd[2];
  {
    const int col = 16 * (q & 1) + (lane & 15), j0 = 16 * (q >> 1) + 4 * (lane >> 4);
    const int kb = col < 8 ? col : col < 12 ? 256 + (col - 8) : col < 16 ? 384 + 64 * ((col - 12) >> 1) + ((col - 12) & 1) : col < 20 ? 512 + 32 * (col - 16) : 640;
    const int kst = col < 8 ? 8 : col < 12 ? 4 : col < 16 ? 2 : col < 20 ? 1 : 0;
    unsigned dd[4];
#pragma unroll
    for (int r = 0; r < 4; ++r) dd[r] = (unsigned)(col < 20 ? kb + kst * (j0 + r) : (col == 20 && j0 + r == 0) ? 640 : 642) * 4u;
    ktd[0] = dd[0] | (dd[1] << 16); ktd[1] = dd[2] | (dd[3] << 16);
    asm volatile("" : "+v"(ktd[0]), "+v"(ktd[1]));
  }
  if (tid < 320) *(u32x4 *)(sm + CV_U + (tid / 160) * 7680 + (tid % 160) * 16) = u32x4{0, 0, 0, 0};
  else if (tid < 320 + 64) { const int r = tid - 320; *(u32x2 *)(sm + CV_KC + (r >> 3) * KC_STRIDE + (513 + (r & 7)) * 8) = u32x2{0, 0}; }
  else if (tid < 320 + 64 + 2) *(float *)(sm + CV_KT + (tid - 384) * 644 * 4 + 641 * 4) = 0.f;
  SCHED0();
  {
    float f[8];
#pragma unroll
    for (int i = 0; i < 4; ++i) { f[2 * i] = bflo(xrow[i]); f[2 * i + 1] = bfhi(xrow[i]); }
    const float s = ((f[0] + f[1]) + (f[2] + f[3])) + ((f[4] + f[5]) + (f[6] + f[7]));
    const float mean = wave_sum(s) * (1.0f / D_);
    float qq = 0;
#pragma unroll
    for (int i = 0; i < 8; ++i) { const float dl = f[i] - mean; qq = __builtin_fmaf(dl, dl, qq); }
    const float var = wave_sum(qq) * (1.0f / D_);
    STAMP(28);
    const float m = bfr(mean), sd = bfr(__builtin_amdgcn_sqrtf(bfr(bfr(var) + 1e-5f)));
    const float rs = 1.0f / sd;
    u32x4 uo;
#pragma unroll
    for (int i = 0; i < 4; ++i) uo[i] = pack_bf16(bfr(f[2 * i] - m) * rs, bfr(f[2 * i + 1] - m) * rs);
    *(u32x4 *)(sm + CV_T + (w * 260 + 4 * lane) * 4) = uo;
    *(u32x4 *)(sm + CV_T + 8320 + (w * 260 + 4 * lane) * 4) = xrow;
  }
  __syncthreads();
  asm volatile("" : "+v"(E));
  const unsigned tag = (unsigned)__builtin_amdgcn_readfirstlane((int)E) + 1u;
  {
    const unsigned *ut = (const unsigned *)(sm + CV_T);
    const int k = lane & 7, P0 = 32 * w + (lane >> 3);
#pragma unroll
    for (int i = 0; i < 4; ++i) {
      const int P = P0 + 8 * i;
      st128s(ws, WS_UG + (unsigned)(P * 2048 + 8 * b + k) * 16u, u32x4{ut[k * 260 + P], tag, ut[2080 + k * 260 + P], tag});
    }
  }
  SCHED0();
  STAMP(2);
  unsigned pf = 0;
  if (w == 3) {
    unsigned long long pc; unsigned len;
    asm volatile("s_getpc_b64 %0\n.Lpf_here%=:\n\ts_mov_b32 %1, k_all_end_marker-.Lpf_here%=" : "=s"(pc), "=s"(len));
    const GAS unsigned *cp = (const GAS unsigned *)pc;
    const unsigned off = (unsigned)((b >> 3) * 8 + (lane & 7) + 256 * (lane >> 3)) * 128u;
    if (off + 4 <= len && lane < 16) pf = cp[off >> 2];
  }
  SCHED0();
  const float dt = bfr(fast_exp(bf2f(p_logdt)));
  float S;
  {
    const float c0r = bf2f(p_c0r) * dt, c0i = bf2f(p_c0i) * dt, c1r = bf2f(p_c1r) * dt, c1i = bf2f(p_c1i) * dt;
    float mx = fmaxf(fmaxf(fabsf(c0r), fabsf(c0i)), fmaxf(fabsf(c1r), fabsf(c1i)));
    mx = wave_max(mx);
    int e = 0;
    if (mx > 0.f && mx < 3.0e38f) e = __builtin_amdgcn_frexp_expf(mx);
    e = e < -100 ? -100 : (e > 100 ? 100 : e);
    S = __builtin_amdgcn_ldexpf(1.0f, -e);
    const cplx C = (q & 1) ? cplx{c1r * S, -c1i * S} : cplx{c0r * S, -c0i * S};
    const float Are = -bfr(fast_exp(bf2f(p_logA)));
    const float ar = Are * dt, ai = bf2f(p_aim) * dt;
    const float er = ar * 1.4426950408889634f, tr = ai * 0.15915494309189535f;
    const int jh = q >> 1;
    unsigned *Rim = (unsigned *)(sm + CV_R + ch * 32 * 528) + nn;
    unsigned *Lim = (unsigned *)(sm + CV_L + ch * 32 * 528) + nn;
    const cplx step = zpow(er, tr, 8.0f);
    const cplx step2 = cmul(step, step);
    cplx ca = zpow(er, tr, 128.0f * jh);
    cplx cb = cmul(ca, step);
#pragma unroll
    for (int jj = 0; jj < 16; jj += 2) {
      Rim[(16 * jh + jj) * 132] = pack_f16(ca.r, ca.i);
      Rim[(16 * jh + jj + 1) * 132] = pack_f16(cb.r, cb.i);
      if (jj + 2 < 16) { ca = cmul(ca, step2); cb = cmul(cb, step2); }
    }
    if (jh == 0) {
      const cplx z1 = zpow(er, -tr, 1.0f), z256 = zpow(er, -tr, 256.0f);
      const cplx z2 = cmul(z1, z1);
      cplx ge = C, go = cmul(C, z1);
#pragma unroll
      for (int r = 0; r < 8; r += 2) {
        Lim[r * 132] = pack_f16(ge.r, ge.i);
        Lim[(r + 1) * 132] = pack_f16(go.r, go.i);
        const cplx t = cmul(ge, z256);
        Lim[(8 + (r >> 1)) * 132] = pack_f16(t.r, t.i);
        if (r + 2 < 8) { ge = cmul(ge, z2); go = cmul(go, z2); }
      }
    } else {
      const cplx g4 = cmul(C, zpow(er, -tr, 4.0f));
      const cplx z256 = zpow(er, -tr, 256.0f), z512 = zpow(er, -tr, 512.0f), z1024 = zpow(er, -tr, 1024.0f);
      const cplx z768 = cmul(z512, z256);
      cplx t;
      t = cmul(C, z512);  Lim[12 * 132] = pack_f16(t.r, t.i);
      t = cmul(g4, z512); Lim[13 * 132] = pack_f16(t.r, t.i);
      t = cmul(C, z768);  Lim[14 * 132] = pack_f16(t.r, t.i);
      t = cmul(g4, z768); Lim[15 * 132] = pack_f16(t.r, t.i);
      t = cmul(C, z1024);
#pragma unroll
      for (int i = 0; i < 5; ++i) {
        Lim[(16 + i) * 132] = pack_f16(t.r, t.i);
        if (i < 4) t = cmul(t, z256);
      }
    }
  }
  STAMP(3);
  __syncthreads();
  {
    const unsigned char *Rb = sm + CV_R + ch * 32 * 528 + (16 * (q >> 1) + (lane & 15)) * 528 + (lane >> 4) * 16;
    const unsigned char *Lb = sm + CV_L + ch * 32 * 528 + (16 * (q & 1) + (lane & 15)) * 528 + (lane >> 4) * 16;
    u32x4 ra[8], la[8];
#pragma unroll
    for (int s = 0; s < 8; ++s) { ra[s] = *(const u32x4 *)(Rb + s * 64); la[s] = *(const u32x4 *)(Lb + s * 64); }
    SCHED0();
    f32x4 acc = {0, 0, 0, 0}, acc2 = {0, 0, 0, 0};
#pragma unroll
    for (int s = 0; s < 8; s += 2) { acc = mfma16_f16(ra[s], la[s], acc); acc2 = mfma16_f16(ra[s + 1], la[s + 1], acc2); }
    unsigned char *Kt = sm + CV_KT + ch * 644 * 4;
    *(float *)(Kt + (ktd[0] & 0xffffu)) = 2.0f * (acc[0] + acc2[0]);
    *(float *)(Kt + (ktd[0] >> 16)) = 2.0f * (acc[1] + acc2[1]);
    *(float *)(Kt + (ktd[1] & 0xffffu)) = 2.0f * (acc[2] + acc2[2]);
    *(float *)(Kt + (ktd[1] >> 16)) = 2.0f * (acc[3] + acc2[3]);
  }
  if (w < 2) { wstore(wv, wdst, wstr); DRAIN(); }
  STAMP(4);
  __syncthreads();
  if (tid < 8) st32s(ws, WS_FW + (unsigned)(tid * 256 + b) * 4u, tag);
  {
    float v[2][7];
#pragma unroll
    for (int c2 = 0; c2 < 2; ++c2)
#pragma unroll
      for (int i = 0; i < 7; ++i) v[c2][i] = *(const float *)(sm + CV_KT + c2 * 644 * 4 + ((kix[i >> 1] >> (16 * (i & 1))) & 0xffffu));
#pragma unroll
    for (int c2 = 0; c2 < 2; ++c2) {
      unsigned char *base = sm + CV_KC + c2 * 4 * KC_STRIDE + (tid + 1) * 8;
      const unsigned p01 = pack_f16(v[c2][0], v[c2][1]), p12 = pack_f16(v[c2][1], v[c2][2]), p23 = pack_f16(v[c2][2], v[c2][3]);
      const unsigned p34 = pack_f16(v[c2][3], v[c2][4]), p45 = pack_f16(v[c2][4], v[c2][5]), p56 = pack_f16(v[c2][5], v[c2][6]);
      *(u32x2 *)(base + 0 * KC_STRIDE) = u32x2{p34, p56};
      *(u32x2 *)(base + 1 * KC_STRIDE) = u32x2{p23, p45};
      *(u32x2 *)(base + 2 * KC_STRIDE) = u32x2{p12, p34};
      *(u32x2 *)(base + 3 * KC_STRIDE) = u32x2{p01, p23};
    }
  }
  STAMP(5);
  {
    const unsigned o = WS_UG + (unsigned)(pr * 2048 + 256 * w + lane) * 16u;
    u32x4 g[4];
    for (unsigned spins = 0;;) {
      MEMBAR();
#pragma unroll
      for (int i = 0; i < 4; ++i) g[i] = ld128s(ws, o + 1024 * i);
      bool ok = true;
#pragma unroll
      for (int i = 0; i < 4; ++i) ok = ok && g[i][1] == tag && g[i][3] == tag;
      if (__all(ok)) break;
      if (spin_fail(spins, ws, tag)) break;
    }
    MEMBAR();
    const unsigned sel = (lane & 1) ? 0x03020706u : 0x05040100u;
    const int tok0 = 256 * w + (lane & ~1), cn = lane & 1;
#pragma unroll
    for (int i = 0; i < 4; ++i) {
      const unsigned uo = g[i][0], xo = g[i][2];
      const unsigned un = (unsigned)__builtin_amdgcn_update_dpp(0, (int)uo, 0xB1, 0xf, 0xf, true);
      const unsigned xn = (unsigned)__builtin_amdgcn_update_dpp(0, (int)xo, 0xB1, 0xf, 0xf, true);
      const unsigned uv = __builtin_amdgcn_perm(un, uo, sel), xv = __builtin_amdgcn_perm(xn, xo, sel);
      const int tk = tok0 + 64 * i, a = tk >> 5, bb = tk & 31;
      *(unsigned *)(sm + CV_U + cn * 7680 + ((a + 32) * 40 + bb) * 2) = pack_f16(bflo(uv), bfhi(uv));
      *(unsigned *)(sm + CV_X + cn * 4608 + (a * 36 + bb) * 2) = xv;
    }
  }
  const int T = 4 * (b & 7) + (b >> 6), j = (b >> 3) & 7;
  const int tt = 2 * T + (j >> 2), c = j & 3;
  const int t = lane & 31, h = lane >> 5;
  const int mi = w & 3, kh = w >> 2;
  u32x4 fwv = ld128s(ws, WS_FW + (unsigned)(b & 7) * 1024u + lane * 16);
  __syncthreads();
  STAMP(6);
  if (w >= 2) wissue(512 + 7 * b + (w - 2), wv, wdst, wstr);
  if (w == 2) wissue(512 + 7 * b + 6, wv2, wdst2, wstr2);
  SCHED0();
  u32x4 A1[16], A2[16];
  const unsigned o1 = WS_W1T + (unsigned)(((4 * c + mi) * 32 + 16 * kh) * 64 + lane) * 16u;
  const unsigned o2 = WS_W2T + (unsigned)(((4 * c + mi) * 32 + 16 * kh) * 64 + lane) * 16u;
  f32x16 acc0, acc1;
#pragma unroll
  for (int i = 0; i < 16; ++i) { acc0[i] = 0.f; acc1[i] = 0.f; }
  {
    const int m = lane & 31, cc = m & 3, mu = m >> 2;
    LAS const unsigned char *pl = (LAS const unsigned char *)(sm + CV_KC + ch * 4 * KC_STRIDE + cc * KC_STRIDE + (2048 - 4 * mu + 8 * h) * 2 - 32 * (16 * q + 78));
    LAS const unsigned char *ph = pl + 8;
    LAS const unsigned char *pb = (LAS const unsigned char *)(sm + CV_U + ch * 7680 + ((m + 32 - 8 * q - 7) * 40 + 8 * h) * 2);
    asm volatile("" : "+v"(pl)); asm volatile("" : "+v"(ph));
    u32x4 fa0[2], fa1[2], fb0[2], fb1[2];
    auto ldstep = [&](int i, int bufi) {
      const int dd = i >> 1, s = i & 1;
      const int o0 = 32 * (78 - 2 * dd + s), oo1 = 32 * (14 - 2 * dd + s);
      const u32x2 a0l = *(LAS const u32x2 *)(pl + o0); MEMBAR();
      const u32x2 a0h = *(LAS const u32x2 *)(ph + o0); MEMBAR();
      fb0[bufi] = *(LAS const u32x4 *)(pb + (7 - dd) * 80 + s * 32);
      fb1[bufi] = *(LAS const u32x4 *)(pb + (39 - dd) * 80 + s * 32); MEMBAR();
      const u32x2 a1l = *(LAS const u32x2 *)(pl + oo1); MEMBAR();
      const u32x2 a1h = *(LAS const u32x2 *)(ph + oo1); MEMBAR();
      fa0[bufi] = u32x4{a0l[0], a0l[1], a0h[0], a0h[1]};
      fa1[bufi] = u32x4{a1l[0], a1l[1], a1h[0], a1h[1]};
    };
    ldstep(0, 0);
#pragma unroll
    for (int i = 0; i < 16; ++i) {
      if (i + 1 < 16) ldstep(i + 1, (i + 1) & 1);
      if (i == 4) {
        const bool ok = fwv[0] == tag && fwv[1] == tag && fwv[2] == tag && fwv[3] == tag;
        if (!__all(ok)) wait_flags256(ws, WS_FW + (unsigned)(b & 7) * 1024u, tag, lane);
      }
      if (i >= 4) {
        const int n0 = (i - 4) * 4 / 3, n1 = (i - 3) * 4 / 3;
#pragma unroll
        for (int n = n0; n < n1; ++n) A1[n] = ld128s(ws, o1 + n * 1024);
      }
      SCHED0();
      acc0 = mfma_f16(fa0[i & 1], fb0[i & 1], acc0);
      acc1 = mfma_f16(fa0[i & 1], fb1[i & 1], acc1);
      acc1 = mfma_f16(fa1[i & 1], fb0[i & 1], acc1);
      SCHED0();
    }
  }
  STAMP(7);
  if (w >= 2) wstore(wv, wdst, wstr);
  if (w == 2) wstore(wv2, wdst2, wstr2);
  asm volatile("" ::"v"(pf));
  __syncthreads();
  STAMP(8);
  {
    float *red = (float *)(sm + CV_Z);
#pragma unroll
    for (int k = 0; k < 4; ++k) {
      if (k == q) continue;
      const f32x16 &a = (k >> 1) ? acc1 : acc0;
      const int o = 8 * (k & 1);
      *(f32x4 *)(red + ((((ch * 4 + k) * 4 + q) * 2 + 0) * 64 + lane) * 4) = f32x4{a[o], a[o + 1], a[o + 2], a[o + 3]};
      *(f32x4 *)(red + ((((ch * 4 + k) * 4 + q) * 2 + 1) * 64 + lane) * 4) = f32x4{a[o + 4], a[o + 5], a[o + 6], a[o + 7]};
    }
  }
  __syncthreads();
  {
    const float *red = (const float *)(sm + CV_Z);
    float y[8];
    const int o = 8 * (q & 1);
#pragma unroll
    for (int i = 0; i < 8; ++i) y[i] = (q >> 1) ? acc1[o + i] : acc0[o + i];
#pragma unroll
    for (int k = 0; k < 4; ++k) {
      if (k == q) continue;
      const f32x4 va = *(const f32x4 *)(red + ((((ch * 4 + q) * 4 + k) * 2 + 0) * 64 + lane) * 4);
      const f32x4 vb = *(const f32x4 *)(red + ((((ch * 4 + q) * 4 + k) * 2 + 1) * 64 + lane) * 4);
#pragma unroll
      for (int i = 0; i < 4; ++i) { y[i] += va[i]; y[4 + i] += vb[i]; }
    }
    const float invS = __builtin_amdgcn_ldexpf(1.0f, 254 - (int)(asu(S) >> 23) - 127), Dk = bf2f(p_dsk);
    const int a = 32 * (q >> 1) + (lane & 31), rh = q & 1;
    u16 *Hout = (u16 *)(sm + CV_KT) + ch * 2048;
#pragma unroll
    for (int g = 0; g < 2; ++g) {
      const int bb = 16 * rh + 8 * g + 4 * h;
      const u32x2 uu = *(const u32x2 *)(sm + CV_U + ch * 7680 + ((a + 32) * 40 + bb) * 2);
      const u32x2 xx = *(const u32x2 *)(sm + CV_X + ch * 4608 + (a * 36 + bb) * 2);
      const unsigned uu0 = uu[0], uu1 = uu[1];
      const f16x2 ua = __builtin_bit_cast(f16x2, uu0), ub = __builtin_bit_cast(f16x2, uu1);
      const float uv[4] = {(float)ua[0], (float)ua[1], (float)ub[0], (float)ub[1]};
      const float xv[4] = {bflo(xx[0]), bfhi(xx[0]), bflo(xx[1]), bfhi(xx[1])};
      float hv[4];
#pragma unroll
      for (int i = 0; i < 4; ++i) hv[i] = xv[i] + bfr(bfr(y[4 * g + i] * invS) + bfr(uv[i] * Dk));
      *(u32x2 *)(Hout + 32 * a + bb) = u32x2{pack_bf16(hv[0], hv[1]), pack_bf16(hv[2], hv[3])};
    }
  }
  __syncthreads();
  {
    const u16 *Hout = (const u16 *)(sm + CV_KT);
    const u32x2 a = *(const u32x2 *)(Hout + 4 * tid), cz = *(const u32x2 *)(Hout + 2048 + 4 * tid);
    u32x4 o;
    o[0] = __builtin_amdgcn_perm(cz[0], a[0], 0x05040100u);
    o[1] = __builtin_amdgcn_perm(cz[0], a[0], 0x07060302u);
    o[2] = __builtin_amdgcn_perm(cz[1], a[1], 0x05040100u);
    o[3] = __builtin_amdgcn_perm(cz[1], a[1], 0x07060302u);
    st128s(ws, WS_HP + (unsigned)(pr * 2048 + 4 * tid) * 4u, o);
  }
  STAMP(9);
  DRAIN();
  __syncthreads();
  if (tid < 8) st32s(ws, WS_FZ + (unsigned)(tid * 256 + b) * 4u, tag);
#pragma unroll
  for (int s = 0; s < 16; ++s) A2[s] = ld128s(ws, o2 + s * 1024);
  SCHED0();
  STAMP(10);
  f32x16 cs1;
  {
#pragma unroll
    for (int i = 0; i < 16; ++i) cs1[i] = 0.f;
    const u32x4 ones = {0x3f803f80u, 0x3f803f80u, 0x3f803f80u, 0x3f803f80u};
#pragma unroll
    for (int s = 0; s < 16; ++s) cs1 = mfma_bf16(A1[s], ones, cs1);
  }
  const int sh = (c == 0) ? 1 : (c == 1) ? -1 : (c == 2) ? 2 : -2;
  const int tokS = 32 * tt - sh + t;
  const bool validS = tokS >= 0 && tokS < L_;
  const int tokSc = validS ? tokS : 0;
  if (w == 7) wait_flags256(ws, WS_FZ + (unsigned)(b & 7) * 1024u, tag, lane);
  __syncthreads();
  STAMP(11);
  {
    u32x4 hv[4];
#pragma unroll
    for (int ks = 0; ks < 4; ++ks)
#pragma unroll
      for (int i = 0; i < 4; ++i) hv[ks][i] = ld32s(ws, WS_HP + (unsigned)((8 * (4 * w + ks) + 4 * h + i) * 2048 + tokSc) * 4u);
    float s1 = 0.f, s2 = 0.f;
#pragma unroll
    for (int ks = 0; ks < 4; ++ks) {
      *(u32x4 *)(sm + G_B + ((4 * w + ks) * 64 + lane) * 16) = hv[ks];
#pragma unroll
      for (int i = 0; i < 4; ++i) { s1 = dot2bf(hv[ks][i], 0x3f803f80u, s1); s2 = dot2bf(hv[ks][i], hv[ks][i], s2); }
    }
    s1 = half_sum(s1); s2 = half_sum(s2);
    if (h == 0) *(f32x2 *)(sm + G_ST + (w * 32 + t) * 8) = f32x2{s1, s2};
  }
  __syncthreads();
  STAMP(12);
  {
    f32x16 acc;
#pragma unroll
    for (int i = 0; i < 16; ++i) acc[i] = 0.f;
    u32x4 bf[4];
#pragma unroll
    for (int s = 0; s < 4; ++s) bf[s] = *(const u32x4 *)(sm + G_B + ((16 * kh + s) * 64 + lane) * 16);
    f32x2 sv[8];
#pragma unroll
    for (int i = 0; i < 8; ++i) sv[i] = *(const f32x2 *)(sm + G_ST + (i * 32 + t) * 8);
    SCHED0();
#pragma unroll
    for (int s = 0; s < 16; ++s) {
      const u32x4 cur = bf[s & 3];
      if (s + 4 < 16) bf[s & 3] = *(const u32x4 *)(sm + G_B + ((16 * kh + s + 4) * 64 + lane) * 16);
      acc = mfma_bf16(A1[s], cur, acc);
    }
    const float s1 = ((sv[0][0] + sv[1][0]) + (sv[2][0] + sv[3][0])) + ((sv[4][0] + sv[5][0]) + (sv[6][0] + sv[7][0]));
    const float s2 = ((sv[0][1] + sv[1][1]) + (sv[2][1] + sv[3][1])) + ((sv[4][1] + sv[5][1]) + (sv[6][1] + sv[7][1]));
    float m1, sd1, mean, var;
    row_stats(s1, s2, m1, sd1, mean, var);
    const float r1 = rcp(sd1);
    const float mean2 = (mean - m1) * r1, var2 = var * r1 * r1;
    const float m2 = bfr(mean2), sd2 = bfr(__builtin_amdgcn_sqrtf(bfr(bfr(var2) + 1e-5f)));
    const float alpha = r1 * rcp(sd2), mu = __builtin_fmaf(m2, sd1, m1);
#pragma unroll
    for (int s = 0; s < 16; ++s) { SGB(M_MFMA, 1); SGB(M_DS_RD, 1); SGB(M_VALU | M_TRANS, 4); }
    SCHED0();
    if (w == 0 && h == 0) st64s(ws, WS_ST1 + (unsigned)((tt * 4 + c) * 32 + t) * 8u, u32x2{asu(m1), asu(sd1)});
    float *ex = (float *)(sm + G_EX);
    f32x4 keep0, keep1, give0, give1;
#pragma unroll
    for (int i = 0; i < 4; ++i) {
      give0[i] = __builtin_fmaf(-mu, kh ? cs1[i] : cs1[8 + i], kh ? acc[i] : acc[8 + i]);
      give1[i] = __builtin_fmaf(-mu, kh ? cs1[4 + i] : cs1[12 + i], kh ? acc[4 + i] : acc[12 + i]);
    }
    *(f32x4 *)(ex + ((w * 2 + 0) * 64 + lane) * 4) = give0;
    *(f32x4 *)(ex + ((w * 2 + 1) * 64 + lane) * 4) = give1;
#pragma unroll
    for (int i = 0; i < 4; ++i) {
      keep0[i] = __builtin_fmaf(-mu, kh ? cs1[8 + i] : cs1[i], kh ? acc[8 + i] : acc[i]);
      keep1[i] = __builtin_fmaf(-mu, kh ? cs1[12 + i] : cs1[4 + i], kh ? acc[12 + i] : acc[4 + i]);
    }
    __syncthreads();
    const int pw = w ^ 4;
    const f32x4 va = *(const f32x4 *)(ex + ((pw * 2 + 0) * 64 + lane) * 4);
    const f32x4 vb = *(const f32x4 *)(ex + ((pw * 2 + 1) * 64 + lane) * 4);
    const float az = validS ? alpha : 0.f;
    float o[8];
#pragma unroll
    for (int i = 0; i < 4; ++i) { o[i] = gelu_tanh(az * (keep0[i] + va[i])); o[4 + i] = gelu_tanh(az * (keep1[i] + vb[i])); }
    const u32x4 pk = {pack_bf16(o[0], o[1]), pack_bf16(o[2], o[3]), pack_bf16(o[4], o[5]), pack_bf16(o[6], o[7])};
    const int kstep = 8 * c + 2 * mi + kh;
    st128s(ws, WS_H1 + (unsigned)((2 * kstep + h) * 2048 + 32 * tt + t) * 16u, pk);
  }
  STAMP(13);
  DRAIN();
  __syncthreads();
  if (tid == 0) st32s(ws, WS_F1 + (unsigned)(T * 64 + j) * 4u, tag);
  u32x4 A3[32];
  const unsigned o3 = (w < 7) ? WS_W3T + (unsigned)(((7 * j + w) * 32) * 64 + lane) * 16u : WS_END;
#pragma unroll
  for (int s = 0; s < 16; ++s) A3[s] = ld128s(ws, o3 + s * 1024);
  const int tok2 = 32 * tt + t;
  const int f0 = 128 * c + 32 * mi + 16 * kh + 4 * h;
  SCHED0();
  STAMP(14);
  if (w == 7) wait_flags8(ws, WS_F1 + (unsigned)T * 256u, tag, lane, j & 4, 4);
  __syncthreads();
  STAMP(15);
  {
    u32x4 bv[4];
#pragma unroll
    for (int ks = 0; ks < 4; ++ks) bv[ks] = ld128s(ws, WS_H1 + (unsigned)((2 * (4 * w + ks) + h) * 2048 + tok2) * 16u);
    unsigned hr[4];
    hr[0] = ld32s(ws, WS_HP + (unsigned)((f0 / 2) * 2048 + tok2) * 4u);
    hr[1] = ld32s(ws, WS_HP + (unsigned)((f0 / 2 + 1) * 2048 + tok2) * 4u);
    hr[2] = ld32s(ws, WS_HP + (unsigned)((f0 / 2 + 4) * 2048 + tok2) * 4u);
    hr[3] = ld32s(ws, WS_HP + (unsigned)((f0 / 2 + 5) * 2048 + tok2) * 4u);
    const u32x2 st = ld64s(ws, WS_ST1 + (unsigned)((t < 31) ? ((tt * 4 + 0) * 32 + t + 1) : ((tt * 4 + 1) * 32 + 30)) * 8u);
    SCHED0();
#pragma unroll
    for (int ks = 0; ks < 4; ++ks) *(u32x4 *)(sm + G_B + ((4 * w + ks) * 64 + lane) * 16) = bv[ks];
    __syncthreads();
    STAMP(16);
    f32x16 acc;
#pragma unroll
    for (int i = 0; i < 16; ++i) acc[i] = 0.f;
    u32x4 bf[4];
#pragma unroll
    for (int s = 0; s < 4; ++s) bf[s] = *(const u32x4 *)(sm + G_B + ((16 * kh + s) * 64 + lane) * 16);
    const unsigned st0 = st[0], st1 = st[1];
    const float m1 = asf(st0), rs = rcp(asf(st1));
    float xb[8];
#pragma unroll
    for (int i = 0; i < 4; ++i) { xb[2 * i] = bfr(bfr(bflo(hr[i]) - m1) * rs); xb[2 * i + 1] = bfr(bfr(bfhi(hr[i]) - m1) * rs); }
    SCHED0();
#pragma unroll
    for (int s = 0; s < 16; ++s) {
      const u32x4 cur = bf[s & 3];
      if (s + 4 < 16) bf[s & 3] = *(const u32x4 *)(sm + G_B + ((16 * kh + s + 4) * 64 + lane) * 16);
      acc = mfma_bf16(A2[s], cur, acc);
      if (s >= 8) A3[16 + 2 * (s - 8)] = ld128s(ws, o3 + (16 + 2 * (s - 8)) * 1024), A3[17 + 2 * (s - 8)] = ld128s(ws, o3 + (17 + 2 * (s - 8)) * 1024);
      SCHED0();
    }
    float *ex = (float *)(sm + G_EX);
    *(f32x4 *)(ex + ((w * 2 + 0) * 64 + lane) * 4) = kh ? f32x4{acc[0], acc[1], acc[2], acc[3]} : f32x4{acc[8], acc[9], acc[10], acc[11]};
    *(f32x4 *)(ex + ((w * 2 + 1) * 64 + lane) * 4) = kh ? f32x4{acc[4], acc[5], acc[6], acc[7]} : f32x4{acc[12], acc[13], acc[14], acc[15]};
    __syncthreads();
    const int pw = w ^ 4;
    const f32x4 va = *(const f32x4 *)(ex + ((pw * 2 + 0) * 64 + lane) * 4);
    const f32x4 vb = *(const f32x4 *)(ex + ((pw * 2 + 1) * 64 + lane) * 4);
    float o[8];
#pragma unroll
    for (int i = 0; i < 4; ++i) {
      o[i] = bfr((kh ? acc[8 + i] : acc[i]) + va[i]);
      o[4 + i] = bfr((kh ? acc[12 + i] : acc[4 + i]) + vb[i]);
    }
    const u32x4 pk = {pack_bf16(o[0] + xb[0], o[1] + xb[1]), pack_bf16(o[2] + xb[2], o[3] + xb[3]), pack_bf16(o[4] + xb[4], o[5] + xb[5]), pack_bf16(o[6] + xb[6], o[7] + xb[7])};
    const int kstep = 8 * c + 2 * mi + kh;
    st128s(ws, WS_X1 + (unsigned)((2 * kstep + h) * 2048 + tok2) * 16u, pk);
  }
  STAMP(17);
  DRAIN();
  __syncthreads();
  if (tid == 0) st32s(ws, WS_F2 + (unsigned)(T * 64 + j) * 4u, tag);
  f32x16 cs3;
#pragma unroll
  for (int i = 0; i < 16; ++i) cs3[i] = 0.f;
  {
    const u32x4 ones = {0x3f803f80u, 0x3f803f80u, 0x3f803f80u, 0x3f803f80u};
#pragma unroll
    for (int s = 0; s < 32; ++s) cs3 = mfma_bf16(A3[s], ones, cs3);
  }
  STAMP(18);
  if (w == 7) wait_flags8(ws, WS_F2 + (unsigned)T * 256u, tag, lane, 0, 8);
  __syncthreads();
  STAMP(19);
  {
    const int tl = w >> 2;
    u32x4 bv[8];
#pragma unroll
    for (int ks = 0; ks < 8; ++ks) bv[ks] = ld128s(ws, WS_X1 + (unsigned)((2 * (8 * (w & 3) + ks) + h) * 2048 + 64 * T + 32 * tl + t) * 16u);
    float s1 = 0.f, s2 = 0.f;
#pragma unroll
    for (int ks = 0; ks < 8; ++ks) {
      *(u32x4 *)(sm + G_B + ((tl * 32 + 8 * (w & 3) + ks) * 64 + lane) * 16) = bv[ks];
#pragma unroll
      for (int i = 0; i < 4; ++i) { s1 = dot2bf(bv[ks][i], 0x3f803f80u, s1); s2 = dot2bf(bv[ks][i], bv[ks][i], s2); }
    }
    s1 = half_sum(s1); s2 = half_sum(s2);
    if (h == 0) *(f32x2 *)(sm + G_ST + (w * 32 + t) * 8) = f32x2{s1, s2};
  }
  __syncthreads();
  STAMP(20);
  u32x4 A4[28];
  const unsigned o4 = WS_W4T + (unsigned)(((2 * w) * 112 + 14 * j) * 64 + lane) * 16u;
#define A4_LOAD(n) A4[((n) & 1) * 14 + ((n) >> 1)] = ld128s(ws, o4 + ((((n) & 1) * 112) + ((n) >> 1)) * 1024)
  f32x16 h0, h1;
  float r3[2], m3[2], gl[2];
  const int hoff = (w < 7) ? G_H + (2 * w * 64 + lane) * 16 : G_EX + lane * 16;
  const int hoff1 = (w < 7) ? hoff + 14 * 1024 : hoff;
  {
#pragma unroll
    for (int i = 0; i < 16; ++i) { h0[i] = 0.f; h1[i] = 0.f; }
    u32x4 bw[4];
#pragma unroll
    for (int s = 0; s < 4; ++s) bw[s] = *(const u32x4 *)(sm + G_B + ((0 * 32 + s) * 64 + lane) * 16);
#pragma unroll
    for (int tl = 0; tl < 2; ++tl) {
      float s1 = 0.f, s2 = 0.f;
#pragma unroll
      for (int i = 0; i < 4; ++i) { const f32x2 v = *(const f32x2 *)(sm + G_ST + ((tl * 4 + i) * 32 + t) * 8); s1 += v[0]; s2 += v[1]; }
      float m, sd, mean, var;
      row_stats(s1, s2, m, sd, mean, var);
      r3[tl] = rcp(sd); m3[tl] = m;
      asm volatile("" : "+v"(r3[tl]), "+v"(m3[tl]));
    }
    SCHED0();
#pragma unroll
    for (int s = 0; s < 32; ++s) {
      const u32x4 cur = bw[s & 3];
      bw[s & 3] = *(const u32x4 *)(sm + G_B + ((s + 4) * 64 + lane) * 16);
      if ((s & 3) == 0) A4_LOAD(s >> 2);
      h0 = mfma_bf16(A3[s], cur, h0);
      SCHED0();
    }
    unsigned hp0[8];
#pragma unroll
    for (int s = 0; s < 32; ++s) {
      const u32x4 cur = bw[s & 3];
      if (s + 4 < 32) bw[s & 3] = *(const u32x4 *)(sm + G_B + ((32 + s + 4) * 64 + lane) * 16);
      if (s >= 12) A4_LOAD(8 + (s - 12));
      h1 = mfma_bf16(A3[s], cur, h1);
      if ((s & 1) == 0) {
        const int i = s >> 1;
        gl[i & 1] = gelu_tanh(r3[0] * __builtin_fmaf(-m3[0], cs3[i], h0[i]));
        if (i & 1) hp0[i >> 1] = pack_bf16(gl[0], gl[1]);
        asm volatile("" : "+v"(gl[i & 1]));
      }
      SCHED0();
    }
    *(u32x4 *)(sm + hoff + 0 * 1024) = u32x4{hp0[0], hp0[1], hp0[2], hp0[3]};
    *(u32x4 *)(sm + hoff + 1 * 1024) = u32x4{hp0[4], hp0[5], hp0[6], hp0[7]};
  }
  STAMP(21);
  __syncthreads();
  const unsigned slab = WS_SLAB + (unsigned)j * 2u * 1024u * 1024u;
  {
    f32x16 o00, o10;
#pragma unroll
    for (int i = 0; i < 16; ++i) { o00[i] = 0.f; o10[i] = 0.f; }
    u32x4 bw[3];
#pragma unroll
    for (int s = 0; s < 3; ++s) bw[s] = *(const u32x4 *)(sm + G_H + ((0 * 14 + s) * 64 + lane) * 16);
    unsigned hp1[8];
    SCHED0();
#pragma unroll
    for (int s = 0; s < 14; ++s) {
      const u32x4 cur = bw[s % 3];
      if (s + 3 < 14) bw[s % 3] = *(const u32x4 *)(sm + G_H + ((0 * 14 + s + 3) * 64 + lane) * 16);
      o00 = mfma_bf16(A4[s], cur, o00);
      o10 = mfma_bf16(A4[14 + s], cur, o10);
      if (s < 8) {
        const int i = 2 * s;
        gl[0] = gelu_tanh(r3[1] * __builtin_fmaf(-m3[1], cs3[i], h1[i]));
        gl[1] = gelu_tanh(r3[1] * __builtin_fmaf(-m3[1], cs3[i + 1], h1[i + 1]));
        hp1[s] = pack_bf16(gl[0], gl[1]);
        asm volatile("" : "+v"(hp1[s]));
      }
      if (s == 8) {
        *(u32x4 *)(sm + hoff1 + 0 * 1024) = u32x4{hp1[0], hp1[1], hp1[2], hp1[3]};
        *(u32x4 *)(sm + hoff1 + 1 * 1024) = u32x4{hp1[4], hp1[5], hp1[6], hp1[7]};
      }
      SCHED0();
    }
    STAMP(22);
    __syncthreads();
    f32x16 o01, o11;
#pragma unroll
    for (int i = 0; i < 16; ++i) { o01[i] = 0.f; o11[i] = 0.f; }
#pragma unroll
    for (int s = 0; s < 3; ++s) bw[s] = *(const u32x4 *)(sm + G_H + ((1 * 14 + s) * 64 + lane) * 16);
    SCHED0();
#pragma unroll
    for (int s = 0; s < 14; ++s) {
      const u32x4 cur = bw[s % 3];
      if (s + 3 < 14) bw[s % 3] = *(const u32x4 *)(sm + G_H + ((1 * 14 + s + 3) * 64 + lane) * 16);
      o01 = mfma_bf16(A4[s], cur, o01);
      o11 = mfma_bf16(A4[14 + s], cur, o11);
      if (s >= 2 && s < 10 && (s & 1) == 0) {
        const int k = (s - 2) >> 1, mt = k >> 1, s2 = k & 1;
        const f32x16 &a = mt ? o10 : o00;
        const u32x4 pk = {pack_bf16(a[8 * s2], a[8 * s2 + 1]), pack_bf16(a[8 * s2 + 2], a[8 * s2 + 3]), pack_bf16(a[8 * s2 + 4], a[8 * s2 + 5]), pack_bf16(a[8 * s2 + 6], a[8 * s2 + 7])};
        st128s(ws, slab + (unsigned)((2 * (2 * (2 * w + mt) + s2) + h) * 2048 + 64 * T + t) * 16u, pk);
      }
      SCHED0();
    }
#pragma unroll
    for (int mt = 0; mt < 2; ++mt) {
      const f32x16 &a = mt ? o11 : o01;
#pragma unroll
      for (int s2 = 0; s2 < 2; ++s2) {
        const u32x4 pk = {pack_bf16(a[8 * s2], a[8 * s2 + 1]), pack_bf16(a[8 * s2 + 2], a[8 * s2 + 3]), pack_bf16(a[8 * s2 + 4], a[8 * s2 + 5]), pack_bf16(a[8 * s2 + 6], a[8 * s2 + 7])};
        st128s(ws, slab + (unsigned)((2 * (2 * (2 * w + mt) + s2) + h) * 2048 + 64 * T + 32 + t) * 16u, pk);
      }
    }
  }
  const int fh = tid & 1, fks = (tid >> 1) & 3, ftk = tid >> 3;
  const int ftok = 64 * T + ftk, fkstep = 4 * j + fks, fchunk = 2 * fkstep + fh, ff0 = 16 * fkstep + 4 * fh;
  STAMP(23);
  DRAIN();
  __syncthreads();
  if (tid == 0) st32s(ws, WS_F3 + (unsigned)(T * 64 + j) * 4u, tag);
  unsigned fhv[4];
  fhv[0] = ld32s(ws, WS_HP + (unsigned)((ff0 / 2) * 2048 + ftok) * 4u);
  fhv[1] = ld32s(ws, WS_HP + (unsigned)((ff0 / 2 + 1) * 2048 + ftok) * 4u);
  fhv[2] = ld32s(ws, WS_HP + (unsigned)((ff0 / 2 + 4) * 2048 + ftok) * 4u);
  fhv[3] = ld32s(ws, WS_HP + (unsigned)((ff0 / 2 + 5) * 2048 + ftok) * 4u);
  const u32x4 fx1v = ld128s(ws, WS_X1 + (unsigned)(fchunk * 2048 + ftok) * 16u);
  SCHED0();
  STAMP(24);
  if (w == 7) wait_flags8(ws, WS_F3 + (unsigned)T * 256u, tag, lane, 0, 8);
  __syncthreads();
  STAMP(25);
  {
    u32x4 sl[8];
#pragma unroll
    for (int qq = 0; qq < 8; ++qq) sl[qq] = ld128s(ws, WS_SLAB + (unsigned)qq * 2u * 1024u * 1024u + (unsigned)(fchunk * 2048 + ftok) * 16u);
    float acc[8];
#pragma unroll
    for (int i = 0; i < 4; ++i) { acc[2 * i] = bflo(sl[0][i]); acc[2 * i + 1] = bfhi(sl[0][i]); }
#pragma unroll
    for (int qq = 1; qq < 8; ++qq)
#pragma unroll
      for (int i = 0; i < 4; ++i) { acc[2 * i] += bflo(sl[qq][i]); acc[2 * i + 1] += bfhi(sl[qq][i]); }
    float o[8];
#pragma unroll
    for (int i = 0; i < 4; ++i) {
      o[2 * i] = bflo(fhv[i]) + bfr(bflo(fx1v[i]) + bfr(acc[2 * i]));
      o[2 * i + 1] = bfhi(fhv[i]) + bfr(bfhi(fx1v[i]) + bfr(acc[2 * i + 1]));
    }
    GAS u16 *dst = (GAS u16 *)p.out + (size_t)ftok * D_ + ff0;
    *(GAS u32x2 *)dst = u32x2{pack_bf16(o[0], o[1]), pack_bf16(o[2], o[3])};
    *(GAS u32x2 *)(dst + 8) = u32x2{pack_bf16(o[4], o[5]), pack_bf16(o[6], o[7])};
  }
  if (b == 0 && tid == 0) st32s(ws, WS_EPOCH, tag);
  STAMP(26);
  asm volatile("k_all_end_marker:");
}

extern "C" void kernel_launch(void *const *d_in, const int *in_sizes, int n_in, void *d_out, int out_size, void *d_ws,
                              size_t ws_size, hipStream_t stream) {
  Params p;
  p.x = (const u16 *)d_in[0];
  p.logA = (const u16 *)d_in[1];
  p.Aim = (const u16 *)d_in[2];
  p.Cre = (const u16 *)d_in[3];
  p.Cim = (const u16 *)d_in[4];
  p.logdt = (const u16 *)d_in[5];
  p.Dskip = (const u16 *)d_in[6];
  p.w1 = (const u16 *)d_in[8];
  p.w2 = (const u16 *)d_in[9];
  p.w3 = (const u16 *)d_in[10];
  p.w4 = (const u16 *)d_in[11];
  p.out = (u16 *)d_out;
  p.ws = (unsigned char *)d_ws;
  k_all<<<256, 512, 0, stream>>>(p);
}
```

```cpp
#include <hip/hip_runtime.h>
#include <hip/hip_bf16.h>
#include <stdint.h>

#define WGFLAGS
#define DI __device__ __forceinline__
#define GAS __attribute__((address_space(1)))
#define LAS __attribute__((address_space(3)))
typedef __attribute__((ext_vector_type(8))) short bf16x8;
typedef __attribute__((ext_vector_type(8))) _Float16 f16x8;
typedef __attribute__((ext_vector_type(2))) _Float16 f16x2;
typedef __attribute__((ext_vector_type(2))) __bf16 bf16x2;
typedef __attribute__((ext_vector_type(16))) float f32x16;
typedef __attribute__((ext_vector_type(4))) float f32x4;
typedef __attribute__((ext_vector_type(2))) float f32x2;
typedef __attribute__((ext_vector_type(4))) unsigned u32x4;
typedef __attribute__((ext_vector_type(2))) unsigned u32x2;
typedef unsigned short u16;

constexpr int L_ = 2048, D_ = 512, NS_ = 128;

constexpr unsigned WS_EPOCH = 0;
constexpr unsigned WS_ABORT = 256;
constexpr unsigned WS_FW = 4096;
constexpr unsigned WS_FZ = WS_FW + 8192;
constexpr unsigned WS_F1 = WS_FZ + 8192;
constexpr unsigned WS_F2 = WS_F1 + 8192;
constexpr unsigned WS_F3 = WS_F2 + 8192;
constexpr unsigned WS_ST1 = 65536;
constexpr unsigned WS_W1T = 131072;
constexpr unsigned WS_W2T = WS_W1T + 512 * 1024;
constexpr unsigned WS_W3T = WS_W2T + 512 * 1024;
constexpr unsigned WS_W4T = WS_W3T + 56 * 32 * 1024;
constexpr unsigned WS_UG = WS_W4T + 16 * 112 * 1024;
constexpr unsigned WS_XQ = WS_UG + 4 * 1024 * 1024;
constexpr unsigned WS_HP = WS_XQ + 2 * 1024 * 1024;
constexpr unsigned WS_H1 = WS_HP + 2 * 1024 * 1024;
constexpr unsigned WS_X1 = WS_H1 + 2 * 1024 * 1024;
constexpr unsigned WS_SLAB = WS_X1 + 2 * 1024 * 1024;
constexpr unsigned WS_END = WS_SLAB + 16 * 1024 * 1024;
constexpr int AUX_SC1 = 16;

#define STAMP(i) __builtin_amdgcn_sched_barrier(0)
#define NPASS(i)

DI float asf(unsigned u) { return __builtin_bit_cast(float, u); }
DI unsigned asu(float f) { return __builtin_bit_cast(unsigned, f); }
DI float bf2f(unsigned b) { return asf(b << 16); }
DI float bflo(unsigned w) { return asf(w << 16); }
DI float bfhi(unsigned w) { return asf(w & 0xffff0000u); }
DI float bfr(float x) { return (float)(__bf16)x; }
DI unsigned pack_bf16(float a, float b) {
  f32x2 v = {a, b};
  bf16x2 r = __builtin_convertvector(v, bf16x2);
  return __builtin_bit_cast(unsigned, r);
}
DI unsigned pack_f16(float a, float b) {
  f32x2 v = {a, b};
  f16x2 r = __builtin_convertvector(v, f16x2);
  return __builtin_bit_cast(unsigned, r);
}
DI float rcp(float x) { return __builtin_amdgcn_rcpf(x); }
DI float gelu_tanh(float x) {
  const float c0 = -2.0f * 0.7978845608028654f * 1.4426950408889634f;
  const float c1 = c0 * 0.044715f;
  float t = x * __builtin_fmaf(x * x, c1, c0);
  float e = __builtin_amdgcn_exp2f(t);
  return x * rcp(1.0f + e);
}
DI f32x16 mfma_bf16(u32x4 a, u32x4 b, f32x16 c) {
  return __builtin_amdgcn_mfma_f32_32x32x16_bf16(__builtin_bit_cast(bf16x8, a), __builtin_bit_cast(bf16x8, b), c, 0, 0, 0);
}
DI f32x16 mfma_f16(u32x4 a, u32x4 b, f32x16 c) {
  return __builtin_amdgcn_mfma_f32_32x32x16_f16(__builtin_bit_cast(f16x8, a), __builtin_bit_cast(f16x8, b), c, 0, 0, 0);
}
DI f32x4 mfma16_f16(u32x4 a, u32x4 b, f32x4 c) {
  return __builtin_amdgcn_mfma_f32_16x16x32_f16(__builtin_bit_cast(f16x8, a), __builtin_bit_cast(f16x8, b), c, 0, 0, 0);
}
template <int CTRL, int RM> DI float dpp_add(float v) {
  return v + asf((unsigned)__builtin_amdgcn_update_dpp(0, (int)asu(v), CTRL, RM, 0xf, true));
}
template <int CTRL, int RM> DI float dpp_max(float v) {
  return fmaxf(v, asf((unsigned)__builtin_amdgcn_update_dpp(0, (int)asu(v), CTRL, RM, 0xf, true)));
}
DI float wave_sum(float v) {
  v = dpp_add<0x111, 0xf>(v); v = dpp_add<0x112, 0xf>(v); v = dpp_add<0x114, 0xf>(v); v = dpp_add<0x118, 0xf>(v);
  v = dpp_add<0x142, 0xa>(v); v = dpp_add<0x143, 0xc>(v);
  return asf((unsigned)__builtin_amdgcn_readlane((int)asu(v), 63));
}
DI float wave_max(float v) {
  v = dpp_max<0x111, 0xf>(v); v = dpp_max<0x112, 0xf>(v); v = dpp_max<0x114, 0xf>(v); v = dpp_max<0x118, 0xf>(v);
  v = dpp_max<0x142, 0xa>(v); v = dpp_max<0x143, 0xc>(v);
  return asf((unsigned)__builtin_amdgcn_readlane((int)asu(v), 63));
}
DI float half_sum(float v) {
  const auto r = __builtin_amdgcn_permlane32_swap(asu(v), asu(v), false, false);
  return asf(r[0]) + asf(r[1]);
}
DI float dot2bf(unsigned a, unsigned b, float c) {
  return __builtin_amdgcn_fdot2_f32_bf16(__builtin_bit_cast(bf16x2, a), __builtin_bit_cast(bf16x2, b), c, false);
}
#define DRAIN() asm volatile("s_waitcnt vmcnt(0)" ::: "memory")
#define MEMBAR() asm volatile("" ::: "memory")
#define SCHED0() __builtin_amdgcn_sched_barrier(0)
#define PIN(x) asm volatile("" : "+v"(x))
#define SGB(mask, n) __builtin_amdgcn_sched_group_barrier(mask, n, 0)
constexpr int M_MFMA = 0x8, M_VALU = 0x2, M_VMEM_RD = 0x20, M_VMEM_WR = 0x40, M_DS_RD = 0x100, M_DS_WR = 0x200, M_TRANS = 0x400;

struct Params {
  const u16 *x, *logA, *Aim, *Cre, *Cim, *logdt, *Dskip, *w1, *w2, *w3, *w4;
  u16 *out;
  unsigned char *ws;
};

typedef __amdgpu_buffer_rsrc_t rsrc_t;
DI u32x4 ld128s(rsrc_t r, unsigned off) { return __builtin_amdgcn_raw_buffer_load_b128(r, off, 0, AUX_SC1); }
DI unsigned ld32s(rsrc_t r, unsigned off) { return __builtin_amdgcn_raw_buffer_load_b32(r, off, 0, AUX_SC1); }
DI u32x2 ld64s(rsrc_t r, unsigned off) { return __builtin_amdgcn_raw_buffer_load_b64(r, off, 0, AUX_SC1); }
DI void st128s(rsrc_t r, unsigned off, u32x4 v) { __builtin_amdgcn_raw_buffer_store_b128(v, r, off, 0, AUX_SC1); }
DI void st64s(rsrc_t r, unsigned off, u32x2 v) { __builtin_amdgcn_raw_buffer_store_b64(v, r, off, 0, AUX_SC1); }
DI void st32s(rsrc_t r, unsigned off, unsigned v) { __builtin_amdgcn_raw_buffer_store_b32(v, r, off, 0, AUX_SC1); }

constexpr unsigned SPIN_CAP = 1u << 18;
DI bool spin_fail(unsigned &spins, rsrc_t r, unsigned tag) {
  ++spins;
  if (spins > SPIN_CAP) { st32s(r, WS_ABORT, tag); return true; }
  if ((spins & 255u) == 0u) return __builtin_amdgcn_readfirstlane(ld32s(r, WS_ABORT)) == tag;
  return false;
}
DI bool flags_ok(const u32x4 &v, unsigned tag) { return __all(v[0] == tag && v[1] == tag && v[2] == tag && v[3] == tag); }
DI void wait_flags256(rsrc_t r, unsigned off, unsigned tag, int lane) {
  const unsigned o = off + lane * 16;
  u32x4 v0 = ld128s(r, o); MEMBAR(); __builtin_amdgcn_s_sleep(6);
  u32x4 v1 = ld128s(r, o); MEMBAR(); __builtin_amdgcn_s_sleep(6);
  u32x4 v2 = ld128s(r, o); MEMBAR();
  for (unsigned spins = 0;;) {
    if (flags_ok(v0, tag)) break;
    v0 = ld128s(r, o); MEMBAR();
    if (flags_ok(v1, tag)) break;
    v1 = ld128s(r, o); MEMBAR();
    if (flags_ok(v2, tag)) break;
    v2 = ld128s(r, o); MEMBAR();
    if (spin_fail(spins, r, tag)) break;
  }
  MEMBAR();
}
DI void wait_flags8(rsrc_t r, unsigned off, unsigned tag, int lane, int lo, int n) {
  const bool mine = lane >= lo && lane < lo + n;
  const unsigned o = off + (mine ? lane : lo) * 4;
  unsigned v0 = ld32s(r, o); MEMBAR(); __builtin_amdgcn_s_sleep(6);
  unsigned v1 = ld32s(r, o); MEMBAR(); __builtin_amdgcn_s_sleep(6);
  unsigned v2 = ld32s(r, o); MEMBAR();
  for (unsigned spins = 0;;) {
    if (__all(v0 == tag)) break;
    v0 = ld32s(r, o); MEMBAR();
    if (__all(v1 == tag)) break;
    v1 = ld32s(r, o); MEMBAR();
    if (__all(v2 == tag)) break;
    v2 = ld32s(r, o); MEMBAR();
    if (spin_fail(spins, r, tag)) break;
  }
  MEMBAR();
}

typedef f32x2 cx;
struct cxm { cx b, r; };
DI cxm cxmul(cx b) { return cxm{b, cx{-b.y, b.x}}; }
DI cx cmul(cx a, const cxm &m) { return __builtin_elementwise_fma(a.yy, m.r, a.xx * m.b); }
DI cx zpow(float er, float tr, float pw) {
  const float mag = __builtin_amdgcn_exp2f(er * pw);
  const float ph = __builtin_amdgcn_fractf(tr * pw);
  return cx{mag * __builtin_amdgcn_cosf(ph), mag * __builtin_amdgcn_sinf(ph)};
}
DI unsigned pack_cx(cx v) { return __builtin_bit_cast(unsigned, __builtin_convertvector(v, f16x2)); }
DI float fast_exp(float x) { return __builtin_amdgcn_exp2f(x * 1.4426950408889634f); }

constexpr int CV_U = 0;
constexpr int CV_X = 15360;
constexpr int CV_KT = CV_X + 9216;
constexpr int CV_Z = CV_KT + 8192;
constexpr int CV_R = CV_Z, CV_L = CV_Z + 2 * 32 * 528;
constexpr int KC_STRIDE = 4416;
constexpr int CV_KC = CV_Z + 67584;
constexpr int CV_T = CV_KC + 35328;
constexpr int G_B = 0;
constexpr int G_H = 65536;
constexpr int G_ST = G_H + 28672;
constexpr int G_EX = G_ST + 4096;
constexpr int SM_BYTES = CV_T + 16896;
static_assert(G_EX + 16384 <= SM_BYTES, "lds");

DI void row_stats(float s1, float s2, float &m, float &sd, float &mean, float &var) {
  mean = s1 * (1.0f / D_);
  var = fmaxf(__builtin_fmaf(s2, 1.0f / D_, -mean * mean), 0.f);
  m = bfr(mean);
  sd = bfr(__builtin_amdgcn_sqrtf(bfr(bfr(var) + 1e-5f)));
}

__global__ void __launch_bounds__(512, 2) k_all(Params p) {
  __shared__ __attribute__((aligned(16))) unsigned char sm[SM_BYTES];
  STAMP(0);
  const int tid = threadIdx.x, lane = tid & 63, w = __builtin_amdgcn_readfirstlane(tid >> 6), b = blockIdx.x;
  unsigned long long pw1 = (unsigned long long)p.w1, pw2 = (unsigned long long)p.w2, pw3 = (unsigned long long)p.w3, pw4 = (unsigned long long)p.w4;
  asm volatile("" : "+s"(pw1), "+s"(pw2), "+s"(pw3), "+s"(pw4) : "s"(p.x), "s"(p.logA), "s"(p.Aim), "s"(p.Cre), "s"(p.Cim), "s"(p.logdt), "s"(p.Dskip), "s"(p.out), "s"(p.ws));
  const rsrc_t ws = __builtin_amdgcn_make_buffer_rsrc((void *)p.ws, 0, (int)WS_END, 0x00020000);
  STAMP(27);
  const u32x4 xrow0 = *(const GAS u32x4 *)(p.x + (size_t)(16 * (b >> 1) + 2 * w) * D_ + 8 * lane);
  const u32x4 xrow1 = *(const GAS u32x4 *)(p.x + (size_t)(16 * (b >> 1) + 2 * w + 1) * D_ + 8 * lane);
  unsigned E = ld32s(ws, WS_EPOCH);
  const int pr = (b & 7) * 32 + (b >> 3), d0 = 2 * pr;
  const int ch = w >> 2, q = w & 3;
  const int d = d0 + ch;
  const int nn = 64 * (q & 1) + lane;
  const unsigned p_logdt = ((const GAS u16 *)p.logdt)[d];
  const unsigned p_c0r = ((const GAS u16 *)p.Cre)[d * NS_ + lane], p_c0i = ((const GAS u16 *)p.Cim)[d * NS_ + lane];
  const unsigned p_c1r = ((const GAS u16 *)p.Cre)[d * NS_ + 64 + lane], p_c1i = ((const GAS u16 *)p.Cim)[d * NS_ + 64 + lane];
  const unsigned p_logA = ((const GAS u16 *)p.logA)[d * NS_ + nn], p_aim = ((const GAS u16 *)p.Aim)[d * NS_ + nn];
  const unsigned p_dsk = ((const GAS u16 *)p.Dskip)[d];
  SCHED0();
  auto waddr = [&](int df, const GAS u16 *&src, int &st1, int &st4, unsigned &dst, unsigned &dst2) {
    const bool m1 = df < 256, m2 = df < 512, m3 = df < 1408;
    const unsigned long long W = m1 ? pw1 : m2 ? pw2 : m3 ? pw3 : pw4;
    const int ncols = m3 && !m2 ? 1792 : 512;
    const int rel = m2 ? (df & 255) : m3 ? df - 512 : df - 1408;
    const int mtp = m3 ? rel >> 5 : rel / 112, s = m3 ? rel & 31 : rel % 112, nks = m3 ? 32 : 112;
    const unsigned base = m1 ? WS_W1T : m2 ? WS_W2T : m3 ? WS_W3T : WS_W4T;
    const int c2 = lane & 31, h = lane >> 5;
    const int kh0 = m1 ? 8 * h : 4 * h;
    src = (const GAS u16 *)W + (size_t)(16 * s + kh0) * ncols + 64 * mtp + 2 * c2;
    st1 = ncols; st4 = (m1 ? 4 : 8) * ncols;
    const int m = 2 * (c2 & 15) + (c2 >> 4);
    dst = base + (unsigned)(((2 * mtp * nks + s) * 64 + m + 32 * h) * 16);
    dst2 = (unsigned)nks * 1024u;
  };
  auto wload = [&](const GAS u16 *src, int st1, int st4, int j) { return *(const GAS unsigned *)(src + (j >> 2) * st4 + (j & 3) * st1); };
  auto wstore = [&](const unsigned (&v)[8], unsigned dst, unsigned dst2) {
    u32x4 fa, fb;
#pragma unroll
    for (int i = 0; i < 4; ++i) {
      const unsigned lo = __builtin_amdgcn_perm(v[2 * i + 1], v[2 * i], 0x05040100u), hi = __builtin_amdgcn_perm(v[2 * i + 1], v[2 * i], 0x07060302u);
      const auto r = __builtin_amdgcn_permlane16_swap(lo, hi, false, false);
      fa[i] = r[0]; fb[i] = r[1];
    }
    st128s(ws, dst, fa);
    st128s(ws, dst + dst2, fb);
  };
  unsigned wv[8];
  unsigned wdst = WS_END, wstr = 0;
  if (w < 2) {
    const GAS u16 *src; int st1, st4;
    waddr(2 * b + w, src, st1, st4, wdst, wstr);
#pragma unroll
    for (int j = 0; j < 8; ++j) wv[j] = wload(src, st1, st4, j);
  }
  unsigned tlbw = 0;
  if (w == 5) {
    if (lane < 17) tlbw = ld32s(ws, (unsigned)lane * 2097152u + 2048u);
    else if (lane < 19) tlbw = *(const GAS unsigned *)((const GAS unsigned char *)p.out + (lane - 17) * (2097152 - 4));
  }
  SCHED0();
  STAMP(1);
  unsigned kix[4];
  {
    unsigned t7[8];
#pragma unroll
    for (int i = 0; i < 8; i += 2) {
      const int ta = 2047 - 4 * tid - i, tb = ta - 1;
      const unsigned bits = pack_bf16((float)ta, (float)tb);
      const unsigned ia = ta < 128 ? (unsigned)ta : (bits & 0xffffu) - 17024u, ib = tb < 128 ? (unsigned)tb : (bits >> 16) - 17024u;
      t7[i] = (ta >= 0 ? ia : 641u) * 4u;
      t7[i + 1] = (tb >= 0 ? ib : 641u) * 4u;
    }
#pragma unroll
    for (int i = 0; i < 4; ++i) { kix[i] = t7[2 * i] | (t7[2 * i + 1] << 16); PIN(kix[i]); }
  }
  unsigned ktd[2];
  {
    const int col = 16 * (q & 1) + (lane & 15), j0 = 16 * (q >> 1) + 4 * (lane >> 4);
    const int kb = col < 8 ? col : col < 12 ? 256 + (col - 8) : col < 16 ? 384 + 64 * ((col - 12) >> 1) + ((col - 12) & 1) : col < 20 ? 512 + 32 * (col - 16) : 640;
    const int kst = col < 8 ? 8 : col < 12 ? 4 : col < 16 ? 2 : col < 20 ? 1 : 0;
    unsigned dd[4];
#pragma unroll
    for (int r = 0; r < 4; ++r) dd[r] = (unsigned)(col < 20 ? kb + kst * (j0 + r) : (col == 20 && j0 + r == 0) ? 640 : 642) * 4u;
    ktd[0] = dd[0] | (dd[1] << 16); ktd[1] = dd[2] | (dd[3] << 16);
    PIN(ktd[0]); PIN(ktd[1]);
  }
  if (tid < 320) *(u32x4 *)(sm + CV_U + (tid / 160) * 7680 + (tid % 160) * 16) = u32x4{0, 0, 0, 0};
  else if (tid < 320 + 64) { const int r = tid - 320; *(u32x2 *)(sm + CV_KC + (r >> 3) * KC_STRIDE + (513 + (r & 7)) * 8) = u32x2{0, 0}; }
  else if (tid < 320 + 64 + 2) *(float *)(sm + CV_KT + (tid - 384) * 644 * 4 + 641 * 4) = 0.f;
  SCHED0();
  {
    float s0 = 0.f, s1 = 0.f;
#pragma unroll
    for (int i = 0; i < 4; ++i) { s0 = dot2bf(xrow0[i], 0x3f803f80u, s0); s1 = dot2bf(xrow1[i], 0x3f803f80u, s1); }
    const float mean0 = wave_sum(s0) * (1.0f / D_), mean1 = wave_sum(s1) * (1.0f / D_);
    float q0 = 0.f, q1 = 0.f;
#pragma unroll
    for (int i = 0; i < 4; ++i) {
      const float a = bflo(xrow0[i]) - mean0, c = bfhi(xrow0[i]) - mean0, e = bflo(xrow1[i]) - mean1, g = bfhi(xrow1[i]) - mean1;
      q0 = __builtin_fmaf(a, a, q0); q0 = __builtin_fmaf(c, c, q0); q1 = __builtin_fmaf(e, e, q1); q1 = __builtin_fmaf(g, g, q1);
    }
    const float var0 = wave_sum(q0) * (1.0f / D_), var1 = wave_sum(q1) * (1.0f / D_);
    STAMP(28);
    u32x4 xh;
#pragma unroll
    for (int i = 0; i < 4; ++i) { const auto r = __builtin_amdgcn_permlane32_swap(xrow0[i], xrow1[i], false, false); xh[i] = (b & 1) ? r[1] : r[0]; }
    const float mean = lane < 32 ? mean0 : mean1, var = lane < 32 ? var0 : var1;
    const float m = bfr(mean), sd = bfr(__builtin_amdgcn_sqrtf(bfr(bfr(var) + 1e-5f)));
    const float rs = rcp(sd), mrs = -m * rs;
    u32x4 uo;
#pragma unroll
    for (int i = 0; i < 4; ++i) uo[i] = pack_bf16(__builtin_fmaf(bflo(xh[i]), rs, mrs), __builtin_fmaf(bfhi(xh[i]), rs, mrs));
    unsigned *ut = (unsigned *)(sm + CV_T) + (2 * w + (lane >> 5)) * 132 + 4 * (lane & 31);
    *(u32x4 *)ut = uo;
    *(u32x4 *)(ut + 16 * 132) = xh;
  }
  PIN(E);
  const unsigned tag = (unsigned)__builtin_amdgcn_readfirstlane((int)E) + 1u;
  __syncthreads();
  {
    const unsigned *ut = (const unsigned *)(sm + CV_T);
    const int k2 = lane & 7, tok0 = 16 * (b >> 1) + 2 * k2;
    unsigned ua[2], ub[2];
#pragma unroll
    for (int i = 0; i < 2; ++i) { const int P = 16 * w + 8 * i + (lane >> 3); ua[i] = ut[(2 * k2) * 132 + P]; ub[i] = ut[(2 * k2 + 1) * 132 + P]; }
    const int PP = 8 * w + (lane >> 3);
    const u32x2 xa = *(const u32x2 *)(ut + (16 + 2 * k2) * 132 + 2 * PP), xb = *(const u32x2 *)(ut + (17 + 2 * k2) * 132 + 2 * PP);
#pragma unroll
    for (int i = 0; i < 2; ++i) st128s(ws, WS_UG + (unsigned)((128 * (b & 1) + 16 * w + 8 * i + (lane >> 3)) * 2048 + tok0) * 8u, u32x4{ua[i], tag, ub[i], tag});
    st128s(ws, WS_XQ + (unsigned)((64 * (b & 1) + PP) * 2048 + tok0) * 8u, u32x4{xa[0], xa[1], xb[0], xb[1]});
  }
  if (w < 2) wstore(wv, wdst, wstr);
  SCHED0();
  STAMP(2);
  const GAS u16 *wsrc; int wst1, wst4;
  waddr(512 + 7 * b + (w >= 2 ? w - 2 : 6), wsrc, wst1, wst4, wdst, wstr);
  if (w == 1) wdst = WS_END;
  SCHED0();
  float S;
  {
    const float dt = bfr(fast_exp(bf2f(p_logdt)));
    const float c0r = bf2f(p_c0r), c0i = bf2f(p_c0i), c1r = bf2f(p_c1r), c1i = bf2f(p_c1i);
    const float mx = wave_max(fmaxf(fmaxf(fabsf(c0r), fabsf(c0i)), fmaxf(fabsf(c1r), fabsf(c1i)))) * dt;
    int e = 0;
    if (mx > 0.f && mx < 3.0e38f) e = __builtin_amdgcn_frexp_expf(mx);
    e = e < -100 ? -100 : (e > 100 ? 100 : e);
    S = __builtin_amdgcn_ldexpf(1.0f, -e);
    const cx C = (q & 1) ? cx{c1r * dt * S, -c1i * dt * S} : cx{c0r * dt * S, -c0i * dt * S};
    const float Are = -bfr(fast_exp(bf2f(p_logA)));
    const float er = (Are * dt) * 1.4426950408889634f, tr = (bf2f(p_aim) * dt) * 0.15915494309189535f;
    const int jh = q >> 1;
    unsigned *Rim = (unsigned *)(sm + CV_R + ch * 32 * 528) + nn + 16 * jh * 132;
    unsigned *Lim = (unsigned *)(sm + CV_L + ch * 32 * 528) + nn;
    const cx z8 = zpow(er, tr, 8.0f);
    wv[0] = wload(wsrc, wst1, wst4, 0); wv[1] = wload(wsrc, wst1, wst4, 1);
    SCHED0();
    const cxm m8 = cxmul(z8);
    const cx z16 = cmul(z8, m8);
    const cxm m32 = cxmul(cmul(z16, cxmul(z16)));
    cx r0 = zpow(er, tr, 128.0f * jh);
    cx r1 = cmul(r0, m8), r2 = cmul(r0, cxmul(z16));
    cx r3 = cmul(r2, m8);
    const cx zA = zpow(er, -tr, jh ? 4.0f : 1.0f), z256 = zpow(er, -tr, 256.0f);
    const cxm m256 = cxmul(z256);
#pragma unroll
    for (int jj = 0; jj < 16; jj += 4) {
      if (jj < 12) { wv[2 + (jj >> 1)] = wload(wsrc, wst1, wst4, 2 + (jj >> 1)); wv[3 + (jj >> 1)] = wload(wsrc, wst1, wst4, 3 + (jj >> 1)); SCHED0(); }
      Rim[(jj + 0) * 132] = pack_cx(r0); Rim[(jj + 1) * 132] = pack_cx(r1); Rim[(jj + 2) * 132] = pack_cx(r2); Rim[(jj + 3) * 132] = pack_cx(r3);
      if (jj + 4 < 16) { r0 = cmul(r0, m32); r1 = cmul(r1, m32); r2 = cmul(r2, m32); r3 = cmul(r3, m32); }
    }
    if (jh == 0) {
      const cxm m1 = cxmul(zA);
      const cx z2 = cmul(zA, m1);
      const cxm m2 = cxmul(z2);
      const cxm m4 = cxmul(cmul(z2, m2));
      const cx g1 = cmul(C, m1), g2 = cmul(C, m2), g3 = cmul(g1, m2);
      const cx g4 = cmul(C, m4), g5 = cmul(g1, m4), g6 = cmul(g2, m4), g7 = cmul(g3, m4);
      Lim[0 * 132] = pack_cx(C);  Lim[1 * 132] = pack_cx(g1); Lim[2 * 132] = pack_cx(g2); Lim[3 * 132] = pack_cx(g3);
      Lim[4 * 132] = pack_cx(g4); Lim[5 * 132] = pack_cx(g5); Lim[6 * 132] = pack_cx(g6); Lim[7 * 132] = pack_cx(g7);
      Lim[8 * 132] = pack_cx(cmul(C, m256)); Lim[9 * 132] = pack_cx(cmul(g2, m256)); Lim[10 * 132] = pack_cx(cmul(g4, m256)); Lim[11 * 132] = pack_cx(cmul(g6, m256));
    } else {
      const cx g4 = cmul(C, cxmul(zA));
      const cx z512 = cmul(z256, m256);
      const cxm m512 = cxmul(z512);
      const cx a0 = cmul(C, m512), a1 = cmul(g4, m512);
      const cx b0 = cmul(a0, m512);
      const cx b2 = cmul(b0, m512);
      Lim[12 * 132] = pack_cx(a0); Lim[13 * 132] = pack_cx(a1); Lim[14 * 132] = pack_cx(cmul(a0, m256)); Lim[15 * 132] = pack_cx(cmul(a1, m256));
      Lim[16 * 132] = pack_cx(b0); Lim[17 * 132] = pack_cx(cmul(b0, m256)); Lim[18 * 132] = pack_cx(b2); Lim[19 * 132] = pack_cx(cmul(b2, m256));
      Lim[20 * 132] = pack_cx(cmul(b2, m512));
    }
  }
  STAMP(3);
  __syncthreads();
  {
    const unsigned char *Rb = sm + CV_R + ch * 32 * 528 + (16 * (q >> 1) + (lane & 15)) * 528 + (lane >> 4) * 16;
    const unsigned char *Lb = sm + CV_L + ch * 32 * 528 + (16 * (q & 1) + (lane & 15)) * 528 + (lane >> 4) * 16;
    u32x4 ra[8], la[8];
#pragma unroll
    for (int s = 0; s < 8; ++s) { ra[s] = *(const u32x4 *)(Rb + s * 64); la[s] = *(const u32x4 *)(Lb + s * 64); }
    SCHED0();
    f32x4 acc = {0, 0, 0, 0}, acc2 = {0, 0, 0, 0};
#pragma unroll
    for (int s = 0; s < 8; s += 2) { acc = mfma16_f16(ra[s], la[s], acc); acc2 = mfma16_f16(ra[s + 1], la[s + 1], acc2); }
    unsigned char *Kt = sm + CV_KT + ch * 644 * 4;
    *(float *)(Kt + (ktd[0] & 0xffffu)) = 2.0f * (acc[0] + acc2[0]);
    *(float *)(Kt + (ktd[0] >> 16)) = 2.0f * (acc[1] + acc2[1]);
    *(float *)(Kt + (ktd[1] & 0xffffu)) = 2.0f * (acc[2] + acc2[2]);
    *(float *)(Kt + (ktd[1] >> 16)) = 2.0f * (acc[3] + acc2[3]);
  }
  const unsigned oug = WS_UG + (unsigned)(pr * 2048 + 256 * w + 2 * lane) * 8u;
  asm volatile("s_waitcnt vmcnt(8)" ::: "memory");
  u32x4 ug0 = ld128s(ws, oug), ug1 = ld128s(ws, oug + 1024);
  STAMP(4);
  __syncthreads();
  if (tid < 8) st32s(ws, WS_FW + (unsigned)(tid * 256 + b) * 4u, tag);
  unsigned pf = 0;
  if (w == 3) {
    unsigned long long pc; unsigned len;
    asm volatile("s_getpc_b64 %0\n.Lpf_here%=:\n\ts_mov_b32 %1, k_all_end_marker-.Lpf_here%=" : "=s"(pc), "=s"(len));
    const GAS unsigned *cp = (const GAS unsigned *)pc;
    const unsigned off = (unsigned)((b >> 3) * 8 + (lane & 7) + 256 * (lane >> 3)) * 128u;
    if (off + 4 <= len && lane < 16) pf = cp[off >> 2];
  }
  {
    float v[2][7];
#pragma unroll
    for (int c2 = 0; c2 < 2; ++c2)
#pragma unroll
      for (int i = 0; i < 7; ++i) v[c2][i] = *(const float *)(sm + CV_KT + c2 * 644 * 4 + ((kix[i >> 1] >> (16 * (i & 1))) & 0xffffu));
#pragma unroll
    for (int c2 = 0; c2 < 2; ++c2) {
      unsigned char *base = sm + CV_KC + c2 * 4 * KC_STRIDE + (tid + 1) * 8;
      const unsigned p01 = pack_f16(v[c2][0], v[c2][1]), p12 = pack_f16(v[c2][1], v[c2][2]), p23 = pack_f16(v[c2][2], v[c2][3]);
      const unsigned p34 = pack_f16(v[c2][3], v[c2][4]), p45 = pack_f16(v[c2][4], v[c2][5]), p56 = pack_f16(v[c2][5], v[c2][6]);
      *(u32x2 *)(base + 0 * KC_STRIDE) = u32x2{p34, p56};
      *(u32x2 *)(base + 1 * KC_STRIDE) = u32x2{p23, p45};
      *(u32x2 *)(base + 2 * KC_STRIDE) = u32x2{p12, p34};
      *(u32x2 *)(base + 3 * KC_STRIDE) = u32x2{p01, p23};
    }
  }
  wstore(wv, wdst, wstr);
  STAMP(5);
  {
    for (unsigned spins = 0;;) {
      if (__all(ug0[1] == tag && ug0[3] == tag && ug1[1] == tag && ug1[3] == tag)) break;
      MEMBAR();
      ug0 = ld128s(ws, oug); ug1 = ld128s(ws, oug + 1024);
      if (spin_fail(spins, ws, tag)) break;
      NPASS(30);
    }
    STAMP(29);
    const int tk = 256 * w + 2 * lane;
    unsigned char *ub = sm + CV_U + (((tk >> 5) + 32) * 40 + (tk & 31)) * 2;
    *(unsigned *)(ub) = pack_f16(bflo(ug0[0]), bflo(ug0[2]));
    *(unsigned *)(ub + 7680) = pack_f16(bfhi(ug0[0]), bfhi(ug0[2]));
    *(unsigned *)(ub + 320) = pack_f16(bflo(ug1[0]), bflo(ug1[2]));
    *(unsigned *)(ub + 7680 + 320) = pack_f16(bfhi(ug1[0]), bfhi(ug1[2]));
  }
  const int T = 4 * (b & 7) + (b >> 6), j = (b >> 3) & 7;
  const int tt = 2 * T + (j >> 2), c = j & 3;
  const int t = lane & 31, h = lane >> 5;
  const int mi = w & 3, kh = w >> 2;
  __syncthreads();
  STAMP(6);
  u32x4 A1[16], A2[16];
  const unsigned o1 = WS_W1T + (unsigned)(((4 * c + mi) * 32 + 16 * kh) * 64 + (lane ^ (16 * kh))) * 16u;
  const unsigned o2 = WS_W2T + (unsigned)(((4 * c + mi) * 32 + 16 * kh) * 64 + (lane ^ (16 * kh))) * 16u;
  u32x4 xq[2];
  const unsigned oxq = WS_XQ + (unsigned)((pr >> 1) * 2048 + 4 * tid) * 8u;
  u32x4 fwv;
  f32x16 acc0, acc1;
#pragma unroll
  for (int i = 0; i < 16; ++i) { acc0[i] = 0.f; acc1[i] = 0.f; }
  {
    const int m = lane & 31, cc = m & 3, mu = m >> 2;
    LAS const unsigned char *pl = (LAS const unsigned char *)(sm + CV_KC + ch * 4 * KC_STRIDE + cc * KC_STRIDE + (2048 - 4 * mu + 8 * h) * 2 - 32 * (16 * q + 78));
    LAS const unsigned char *ph = pl + 8;
    LAS const unsigned char *pb = (LAS const unsigned char *)(sm + CV_U + ch * 7680 + ((m + 32 - 8 * q - 7) * 40 + 8 * h) * 2);
    PIN(pl); PIN(ph);
    u32x4 fa0[3], fa1[3], fb0[3], fb1[3];
    auto ldstep = [&](int i, int bufi) {
      const int dd = i >> 1, s = i & 1;
      const int o0 = 32 * (78 - 2 * dd + s), oo1 = 32 * (14 - 2 * dd + s);
      const u32x2 a0l = *(LAS const u32x2 *)(pl + o0); MEMBAR();
      const u32x2 a0h = *(LAS const u32x2 *)(ph + o0); MEMBAR();
      fb0[bufi] = *(LAS const u32x4 *)(pb + (7 - dd) * 80 + s * 32);
      fb1[bufi] = *(LAS const u32x4 *)(pb + (39 - dd) * 80 + s * 32); MEMBAR();
      const u32x2 a1l = *(LAS const u32x2 *)(pl + oo1); MEMBAR();
      const u32x2 a1h = *(LAS const u32x2 *)(ph + oo1); MEMBAR();
      fa0[bufi] = u32x4{a0l[0], a0l[1], a0h[0], a0h[1]};
      fa1[bufi] = u32x4{a1l[0], a1l[1], a1h[0], a1h[1]};
    };
    ldstep(0, 0); ldstep(1, 1);
#pragma unroll
    for (int i = 0; i < 16; ++i) {
      if (i + 2 < 16) ldstep(i + 2, (i + 2) % 3);
      if (i == 2) fwv = ld128s(ws, WS_FW + (unsigned)(b & 7) * 1024u + lane * 16);
      if (i == 5) {
        if (!flags_ok(fwv, tag)) wait_flags256(ws, WS_FW + (unsigned)(b & 7) * 1024u, tag, lane);
        xq[0] = ld128s(ws, oxq); xq[1] = ld128s(ws, oxq + 16);
      }
      if (i >= 6) {
        const int n0 = (i - 6) * 8 / 5, n1 = (i - 5) * 8 / 5;
#pragma unroll
        for (int n = n0; n < n1; ++n) A1[n] = ld128s(ws, o1 + n * 1024);
      }
      acc1 = mfma_f16(fa0[i % 3], fb1[i % 3], acc1);
      acc0 = mfma_f16(fa0[i % 3], fb0[i % 3], acc0);
      acc1 = mfma_f16(fa1[i % 3], fb0[i % 3], acc1);
      SGB(M_MFMA, 1); SGB(M_DS_RD, 2); SGB(M_VMEM_RD, 1); SGB(M_MFMA, 1); SGB(M_DS_RD, 2); SGB(M_VMEM_RD, 1); SGB(M_MFMA, 1); SGB(M_DS_RD, 2); SGB(M_VMEM_RD, 2);
      SCHED0();
    }
  }
  STAMP(7);
  float y[8];
  {
    float *red = (float *)(sm + CV_Z) + (ch * 32 * 64 + lane) * 4 + q * 2 * 64 * 4;
    auto give = [&](int k, const f32x16 &a, int o) {
      *(f32x4 *)(red + (k * 8 + 0) * 256) = f32x4{a[o], a[o + 1], a[o + 2], a[o + 3]};
      *(f32x4 *)(red + (k * 8 + 1) * 256) = f32x4{a[o + 4], a[o + 5], a[o + 6], a[o + 7]};
    };
    auto keep = [&](const f32x16 &a, int o) {
#pragma unroll
      for (int i = 0; i < 8; ++i) y[i] = a[o + i];
    };
    if (q == 0) { give(1, acc0, 8); give(2, acc1, 0); give(3, acc1, 8); keep(acc0, 0); }
    else if (q == 1) { give(0, acc0, 0); give(2, acc1, 0); give(3, acc1, 8); keep(acc0, 8); }
    else if (q == 2) { give(0, acc0, 0); give(1, acc0, 8); give(3, acc1, 8); keep(acc1, 0); }
    else { give(0, acc0, 0); give(1, acc0, 8); give(2, acc1, 0); keep(acc1, 8); }
  }
  {
    const int sel = pr & 1, l0 = 4 * tid, a = l0 >> 5, bb = l0 & 31;
    const unsigned x0 = sel ? xq[0][1] : xq[0][0], x1 = sel ? xq[0][3] : xq[0][2], x2 = sel ? xq[1][1] : xq[1][0], x3 = sel ? xq[1][3] : xq[1][2];
    *(u32x2 *)(sm + CV_X + (a * 36 + bb) * 2) = u32x2{__builtin_amdgcn_perm(x1, x0, 0x05040100u), __builtin_amdgcn_perm(x3, x2, 0x05040100u)};
    *(u32x2 *)(sm + CV_X + 4608 + (a * 36 + bb) * 2) = u32x2{__builtin_amdgcn_perm(x1, x0, 0x07060302u), __builtin_amdgcn_perm(x3, x2, 0x07060302u)};
  }
  asm volatile("" ::"v"(pf), "v"(tlbw));
  __syncthreads();
  STAMP(8);
  {
    const float *red = (const float *)(sm + CV_Z) + (ch * 32 * 64 + lane) * 4 + q * 8 * 256;
    f32x4 va[3], vb[3];
#pragma unroll
    for (int k = 0; k < 3; ++k) {
      const int src = (q + 1 + k) & 3;
      va[k] = *(const f32x4 *)(red + (src * 2 + 0) * 256);
      vb[k] = *(const f32x4 *)(red + (src * 2 + 1) * 256);
    }
    const int a = 32 * (q >> 1) + (lane & 31), bb = 16 * (q & 1) + 4 * h;
    u32x2 uu[2], xx[2];
#pragma unroll
    for (int g = 0; g < 2; ++g) {
      uu[g] = *(const u32x2 *)(sm + CV_U + ch * 7680 + ((a + 32) * 40 + bb + 8 * g) * 2);
      xx[g] = *(const u32x2 *)(sm + CV_X + ch * 4608 + (a * 36 + bb + 8 * g) * 2);
    }
    const float invS = __builtin_amdgcn_ldexpf(1.0f, 127 - (int)(asu(S) >> 23)), Dk = bf2f(p_dsk);
    u16 *Hout = (u16 *)(sm + CV_KT) + ch * 2048 + 32 * a + bb;
#pragma unroll
    for (int g = 0; g < 2; ++g) {
      const f32x4 &r0 = g ? vb[0] : va[0], &r1 = g ? vb[1] : va[1], &r2 = g ? vb[2] : va[2];
      const unsigned uu0 = uu[g][0], uu1 = uu[g][1];
      const f16x2 ua = __builtin_bit_cast(f16x2, uu0), ub = __builtin_bit_cast(f16x2, uu1);
      const float uv[4] = {(float)ua[0], (float)ua[1], (float)ub[0], (float)ub[1]};
      const float xv[4] = {bflo(xx[g][0]), bfhi(xx[g][0]), bflo(xx[g][1]), bfhi(xx[g][1])};
      float hv[4];
#pragma unroll
      for (int i = 0; i < 4; ++i) hv[i] = __builtin_fmaf((y[4 * g + i] + r0[i]) + (r1[i] + r2[i]), invS, __builtin_fmaf(uv[i], Dk, xv[i]));
      *(u32x2 *)(Hout + 8 * g) = u32x2{pack_bf16(hv[0], hv[1]), pack_bf16(hv[2], hv[3])};
    }
  }
  __syncthreads();
  {
    const u16 *Hout = (const u16 *)(sm + CV_KT);
    const u32x2 a = *(const u32x2 *)(Hout + 4 * tid), cz = *(const u32x2 *)(Hout + 2048 + 4 * tid);
    u32x4 o;
    o[0] = __builtin_amdgcn_perm(cz[0], a[0], 0x05040100u);
    o[1] = __builtin_amdgcn_perm(cz[0], a[0], 0x07060302u);
    o[2] = __builtin_amdgcn_perm(cz[1], a[1], 0x05040100u);
    o[3] = __builtin_amdgcn_perm(cz[1], a[1], 0x07060302u);
    st128s(ws, WS_HP + (unsigned)(pr * 2048 + 4 * tid) * 4u, o);
  }
  STAMP(9);
  DRAIN();
  __syncthreads();
  if (tid < 8) st32s(ws, WS_FZ + (unsigned)(tid * 256 + b) * 4u, tag);
  const u32x4 ones = {0x3f803f80u, 0x3f803f80u, 0x3f803f80u, 0x3f803f80u};
  f32x16 cs1;
#pragma unroll
  for (int i = 0; i < 16; ++i) cs1[i] = 0.f;
#pragma unroll
  for (int s = 0; s < 16; ++s) { if (s < 8) A2[s] = ld128s(ws, o2 + s * 1024); cs1 = mfma_bf16(A1[s], ones, cs1); SCHED0(); }
  PIN(cs1);
  STAMP(10);
  const int sh = (c == 0) ? 1 : (c == 1) ? -1 : (c == 2) ? 2 : -2;
  const int tokS = 32 * tt - sh + t;
  const bool validS = tokS >= 0 && tokS < L_;
  const unsigned oh1 = WS_HP + (unsigned)((32 * w + 4 * h) * 2048 + (validS ? tokS : 0)) * 4u;
  u32x4 A3[32];
  const unsigned o3 = (w < 7) ? WS_W3T + (unsigned)(((7 * j + w) * 32) * 64 + lane) * 16u : WS_END;
  if (w == 7) wait_flags256(ws, WS_FZ + (unsigned)(b & 7) * 1024u, tag, lane);
  __syncthreads();
  STAMP(11);
  {
    u32x4 hv[4];
#pragma unroll
    for (int ks = 0; ks < 4; ++ks)
#pragma unroll
      for (int i = 0; i < 4; ++i) hv[ks][i] = __builtin_amdgcn_raw_buffer_load_b32(ws, oh1, (8 * ks + i) * 8192, AUX_SC1);
#pragma unroll
    for (int s = 8; s < 16; ++s) A2[s] = ld128s(ws, o2 + s * 1024);
    SCHED0();
    float s1[2] = {0.f, 0.f}, s2[2] = {0.f, 0.f};
#pragma unroll
    for (int ks = 0; ks < 4; ++ks) {
      *(u32x4 *)(sm + G_B + ((4 * w + ks) * 64 + lane) * 16) = hv[ks];
#pragma unroll
      for (int i = 0; i < 4; ++i) { s1[i & 1] = dot2bf(hv[ks][i], 0x3f803f80u, s1[i & 1]); s2[i & 1] = dot2bf(hv[ks][i], hv[ks][i], s2[i & 1]); }
    }
    const float t1 = half_sum(s1[0] + s1[1]), t2 = half_sum(s2[0] + s2[1]);
    if (h == 0) *(f32x2 *)(sm + G_ST + (w * 32 + t) * 8) = f32x2{t1, t2};
  }
  __syncthreads();
  STAMP(12);
  {
    f32x16 acc;
#pragma unroll
    for (int i = 0; i < 16; ++i) acc[i] = 0.f;
    f32x2 sv[8];
#pragma unroll
    for (int i = 0; i < 8; ++i) sv[i] = *(const f32x2 *)(sm + G_ST + (i * 32 + t) * 8);
    u32x4 bf[4];
#pragma unroll
    for (int s = 0; s < 4; ++s) bf[s] = *(const u32x4 *)(sm + G_B + ((16 * kh + s) * 64 + lane) * 16);
    SCHED0();
#pragma unroll
    for (int s = 0; s < 16; ++s) {
      const u32x4 cur = bf[s & 3];
      if (s + 4 < 16) bf[s & 3] = *(const u32x4 *)(sm + G_B + ((16 * kh + s + 4) * 64 + lane) * 16);
      if ((s & 3) == 3) A3[s >> 2] = ld128s(ws, o3 + (s >> 2) * 1024);
      acc = mfma_bf16(A1[s], cur, acc);
    }
    const f32x2 st = ((sv[0] + sv[1]) + (sv[2] + sv[3])) + ((sv[4] + sv[5]) + (sv[6] + sv[7]));
    float m1, sd1, mean, var;
    row_stats(st[0], st[1], m1, sd1, mean, var);
    const float r1 = rcp(sd1);
    const float mean2 = (mean - m1) * r1, var2 = var * r1 * r1;
    const float m2 = bfr(mean2), sd2 = bfr(__builtin_amdgcn_sqrtf(bfr(bfr(var2) + 1e-5f)));
    float alpha = r1 * rcp(sd2), mu = __builtin_fmaf(m2, sd1, m1);
    alpha = validS ? alpha : 0.f;
#pragma unroll
    for (int s = 0; s < 16; ++s) { SGB(M_MFMA, 1); SGB(M_DS_RD, 1); if ((s & 3) == 3) SGB(M_VMEM_RD, 1); SGB(M_VALU | M_TRANS, 4); }
    PIN(alpha); PIN(mu);
    SCHED0();
    float *ex = (float *)(sm + G_EX);
    f32x4 keep0, keep1, give0, give1;
#pragma unroll
    for (int i = 0; i < 4; ++i) { give0[i] = __builtin_fmaf(-mu, cs1[8 + i], acc[8 + i]); give1[i] = __builtin_fmaf(-mu, cs1[12 + i], acc[12 + i]); }
    *(f32x4 *)(ex + ((w * 2 + 0) * 64 + lane) * 4) = give0;
    *(f32x4 *)(ex + ((w * 2 + 1) * 64 + lane) * 4) = give1;
#pragma unroll
    for (int i = 0; i < 4; ++i) { keep0[i] = __builtin_fmaf(-mu, cs1[i], acc[i]); keep1[i] = __builtin_fmaf(-mu, cs1[4 + i], acc[4 + i]); }
    if (w == 0 && h == 0) st64s(ws, WS_ST1 + (unsigned)((tt * 4 + c) * 32 + t) * 8u, u32x2{asu(m1), asu(r1)});
    __syncthreads();
    const int pw = w ^ 4;
    const f32x4 va = *(const f32x4 *)(ex + ((pw * 2 + 0) * 64 + lane) * 4);
    const f32x4 vb = *(const f32x4 *)(ex + ((pw * 2 + 1) * 64 + lane) * 4);
    float o[8];
#pragma unroll
    for (int i = 0; i < 4; ++i) { o[i] = gelu_tanh(alpha * (keep0[i] + va[i])); o[4 + i] = gelu_tanh(alpha * (keep1[i] + vb[i])); }
    const u32x4 pk = {pack_bf16(o[0], o[1]), pack_bf16(o[2], o[3]), pack_bf16(o[4], o[5]), pack_bf16(o[6], o[7])};
    st128s(ws, WS_H1 + (unsigned)((2 * (8 * c + 2 * mi + kh) + h) * 2048 + 32 * tt + t) * 16u, pk);
  }
  STAMP(13);
  DRAIN();
  __syncthreads();
  if (tid == 0) st32s(ws, WS_F1 + (unsigned)(T * 64 + j) * 4u, tag);
#pragma unroll
  for (int s = 4; s < 12; ++s) A3[s] = ld128s(ws, o3 + s * 1024);
  const int tok2 = 32 * tt + t;
  const int f0 = 128 * c + 32 * mi + 16 * kh + 4 * h;
  unsigned hr[4];
  {
    const unsigned o = WS_HP + (unsigned)((f0 / 2) * 2048 + tok2) * 4u;
    hr[0] = __builtin_amdgcn_raw_buffer_load_b32(ws, o, 0, AUX_SC1);
    hr[1] = __builtin_amdgcn_raw_buffer_load_b32(ws, o, 8192, AUX_SC1);
    hr[2] = __builtin_amdgcn_raw_buffer_load_b32(ws, o, 4 * 8192, AUX_SC1);
    hr[3] = __builtin_amdgcn_raw_buffer_load_b32(ws, o, 5 * 8192, AUX_SC1);
  }
  SCHED0();
  STAMP(14);
  if (w == 7) wait_flags8(ws, WS_F1 + (unsigned)T * 256u, tag, lane, j & 4, 4);
  __syncthreads();
  STAMP(15);
  f32x16 cs3;
  u32x4 A4[28];
  const unsigned o4 = WS_W4T + (unsigned)(((2 * w) * 112 + 14 * j) * 64 + lane) * 16u;
#define A4_LOAD(n) A4[((n) & 1) * 14 + ((n) >> 1)] = __builtin_amdgcn_raw_buffer_load_b128(ws, o4, ((((n) & 1) * 112) + ((n) >> 1)) * 1024, AUX_SC1)
  {
    u32x4 bv[4];
    const unsigned ob = WS_H1 + (unsigned)((8 * w + h) * 2048 + tok2) * 16u;
#pragma unroll
    for (int ks = 0; ks < 4; ++ks) bv[ks] = __builtin_amdgcn_raw_buffer_load_b128(ws, ob, ks * 65536, AUX_SC1);
    const u32x2 st = ld64s(ws, WS_ST1 + (unsigned)((t < 31) ? ((tt * 4 + 0) * 32 + t + 1) : ((tt * 4 + 1) * 32 + 30)) * 8u);
#pragma unroll
    for (int s = 12; s < 20; ++s) A3[s] = ld128s(ws, o3 + s * 1024);
    SCHED0();
#pragma unroll
    for (int i = 0; i < 16; ++i) cs3[i] = 0.f;
#pragma unroll
    for (int s = 0; s < 12; ++s) cs3 = mfma_bf16(A3[s], ones, cs3);
    PIN(cs3);
    SCHED0();
#pragma unroll
    for (int ks = 0; ks < 4; ++ks) *(u32x4 *)(sm + G_B + ((4 * w + ks) * 64 + lane) * 16) = bv[ks];
    __syncthreads();
    STAMP(16);
    f32x16 acc;
#pragma unroll
    for (int i = 0; i < 16; ++i) acc[i] = 0.f;
    u32x4 bf[4];
#pragma unroll
    for (int s = 0; s < 4; ++s) bf[s] = *(const u32x4 *)(sm + G_B + ((16 * kh + s) * 64 + lane) * 16);
    const unsigned st0 = st[0], st1 = st[1];
    const float rs = asf(st1), mrs = -asf(st0) * rs;
    float xb[8];
    SCHED0();
#pragma unroll
    for (int s = 0; s < 16; ++s) {
      const u32x4 cur = bf[s & 3];
      if (s + 4 < 16) bf[s & 3] = *(const u32x4 *)(sm + G_B + ((16 * kh + s + 4) * 64 + lane) * 16);
      acc = mfma_bf16(A2[s], cur, acc);
      if (s < 4) { xb[2 * s] = __builtin_fmaf(bflo(hr[s]), rs, mrs); xb[2 * s + 1] = __builtin_fmaf(bfhi(hr[s]), rs, mrs); PIN(xb[2 * s]); PIN(xb[2 * s + 1]); }
      if ((s & 3) == 3) A3[20 + (s >> 2)] = ld128s(ws, o3 + (20 + (s >> 2)) * 1024);
      SCHED0();
    }
    float *ex = (float *)(sm + G_EX);
    *(f32x4 *)(ex + ((w * 2 + 0) * 64 + lane) * 4) = f32x4{acc[8], acc[9], acc[10], acc[11]};
    *(f32x4 *)(ex + ((w * 2 + 1) * 64 + lane) * 4) = f32x4{acc[12], acc[13], acc[14], acc[15]};
    float o[8];
#pragma unroll
    for (int i = 0; i < 8; ++i) o[i] = acc[i] + xb[i];
    __syncthreads();
    const int pw = w ^ 4;
    const f32x4 va = *(const f32x4 *)(ex + ((pw * 2 + 0) * 64 + lane) * 4);
    const f32x4 vb = *(const f32x4 *)(ex + ((pw * 2 + 1) * 64 + lane) * 4);
    const u32x4 pk = {pack_bf16(o[0] + va[0], o[1] + va[1]), pack_bf16(o[2] + va[2], o[3] + va[3]), pack_bf16(o[4] + vb[0], o[5] + vb[1]), pack_bf16(o[6] + vb[2], o[7] + vb[3])};
    st128s(ws, WS_X1 + (unsigned)((2 * (8 * c + 2 * mi + kh) + h) * 2048 + tok2) * 16u, pk);
  }
  STAMP(17);
  DRAIN();
  __syncthreads();
  if (tid == 0) st32s(ws, WS_F2 + (unsigned)(T * 64 + j) * 4u, tag);
#pragma unroll
  for (int s = 24; s < 32; ++s) A3[s] = ld128s(ws, o3 + s * 1024);
  SCHED0();
#pragma unroll
  for (int s = 12; s < 24; ++s) cs3 = mfma_bf16(A3[s], ones, cs3);
  PIN(cs3);
  STAMP(18);
  if (w == 7) wait_flags8(ws, WS_F2 + (unsigned)T * 256u, tag, lane, 0, 8);
  __syncthreads();
  STAMP(19);
  {
    const int tl = w >> 2;
    u32x4 bv[8];
    const unsigned ob = WS_X1 + (unsigned)((16 * (w & 3) + h) * 2048 + 64 * T + 32 * tl + t) * 16u;
#pragma unroll
    for (int ks = 0; ks < 8; ++ks) bv[ks] = __builtin_amdgcn_raw_buffer_load_b128(ws, ob, ks * 65536, AUX_SC1);
#pragma unroll
    for (int n = 0; n < 4; ++n) A4_LOAD(n);
    SCHED0();
#pragma unroll
    for (int s = 24; s < 32; ++s) cs3 = mfma_bf16(A3[s], ones, cs3);
    PIN(cs3);
    SCHED0();
    float s1[4] = {0.f, 0.f, 0.f, 0.f}, s2[4] = {0.f, 0.f, 0.f, 0.f};
#pragma unroll
    for (int ks = 0; ks < 8; ++ks) {
      *(u32x4 *)(sm + G_B + ((tl * 32 + 8 * (w & 3) + ks) * 64 + lane) * 16) = bv[ks];
#pragma unroll
      for (int i = 0; i < 4; ++i) { s1[i] = dot2bf(bv[ks][i], 0x3f803f80u, s1[i]); s2[i] = dot2bf(bv[ks][i], bv[ks][i], s2[i]); }
    }
    const float t1 = half_sum((s1[0] + s1[1]) + (s1[2] + s1[3])), t2 = half_sum((s2[0] + s2[1]) + (s2[2] + s2[3]));
    if (h == 0) *(f32x2 *)(sm + G_ST + (w * 32 + t) * 8) = f32x2{t1, t2};
  }
  __syncthreads();
  STAMP(20);
  f32x16 h0, h1;
  float r3[2], m3[2];
  const int hoff = (w < 7) ? G_H + (2 * w * 64 + lane) * 16 : G_EX + lane * 16;
  const int hoff1 = (w < 7) ? hoff + 14 * 1024 : hoff;
  {
#pragma unroll
    for (int i = 0; i < 16; ++i) { h0[i] = 0.f; h1[i] = 0.f; }
    f32x2 sv[8];
#pragma unroll
    for (int i = 0; i < 8; ++i) sv[i] = *(const f32x2 *)(sm + G_ST + (i * 32 + t) * 8);
    u32x4 b0[3], b1[3];
#pragma unroll
    for (int s = 0; s < 3; ++s) {
      b0[s] = *(const u32x4 *)(sm + G_B + ((0 * 32 + s) * 64 + lane) * 16);
      b1[s] = *(const u32x4 *)(sm + G_B + ((1 * 32 + s) * 64 + lane) * 16);
    }
    SCHED0();
#pragma unroll
    for (int s = 0; s < 32; ++s) {
      const u32x4 c0 = b0[s % 3], c1 = b1[s % 3];
      if (s + 3 < 32) {
        b0[s % 3] = *(const u32x4 *)(sm + G_B + ((0 * 32 + s + 3) * 64 + lane) * 16);
        b1[s % 3] = *(const u32x4 *)(sm + G_B + ((1 * 32 + s + 3) * 64 + lane) * 16);
      }
      if (s < 24) A4_LOAD(4 + s);
      h0 = mfma_bf16(A3[s], c0, h0);
      h1 = mfma_bf16(A3[s], c1, h1);
      if (s == 1 || s == 3) {
        const int tl = s >> 1;
        const f32x2 st = (sv[4 * tl] + sv[4 * tl + 1]) + (sv[4 * tl + 2] + sv[4 * tl + 3]);
        float m, sd, mean, var;
        row_stats(st[0], st[1], m, sd, mean, var);
        r3[tl] = rcp(sd); m3[tl] = -m;
        PIN(r3[tl]); PIN(m3[tl]);
      }
      SCHED0();
    }
    unsigned hp0[8];
#pragma unroll
    for (int i = 0; i < 8; ++i)
      hp0[i] = pack_bf16(gelu_tanh(r3[0] * __builtin_fmaf(m3[0], cs3[2 * i], h0[2 * i])), gelu_tanh(r3[0] * __builtin_fmaf(m3[0], cs3[2 * i + 1], h0[2 * i + 1])));
    *(u32x4 *)(sm + hoff + 0 * 1024) = u32x4{hp0[0], hp0[1], hp0[2], hp0[3]};
    *(u32x4 *)(sm + hoff + 1 * 1024) = u32x4{hp0[4], hp0[5], hp0[6], hp0[7]};
  }
  STAMP(21);
  __syncthreads();
  const unsigned slab = WS_SLAB + (unsigned)j * 2u * 1024u * 1024u + (unsigned)((8 * w + h) * 2048 + 64 * T + t) * 16u;
  {
    f32x16 o00, o10;
#pragma unroll
    for (int i = 0; i < 16; ++i) { o00[i] = 0.f; o10[i] = 0.f; }
    u32x4 bw[3];
#pragma unroll
    for (int s = 0; s < 3; ++s) bw[s] = *(const u32x4 *)(sm + G_H + ((0 * 14 + s) * 64 + lane) * 16);
    unsigned hp1[8];
    SCHED0();
#pragma unroll
    for (int s = 0; s < 14; ++s) {
      const u32x4 cur = bw[s % 3];
      if (s + 3 < 14) bw[s % 3] = *(const u32x4 *)(sm + G_H + ((0 * 14 + s + 3) * 64 + lane) * 16);
      o00 = mfma_bf16(A4[s], cur, o00);
      o10 = mfma_bf16(A4[14 + s], cur, o10);
      if (s < 8) {
        const int i = 2 * s;
        hp1[s] = pack_bf16(gelu_tanh(r3[1] * __builtin_fmaf(m3[1], cs3[i], h1[i])), gelu_tanh(r3[1] * __builtin_fmaf(m3[1], cs3[i + 1], h1[i + 1])));
        PIN(hp1[s]);
      }
      if (s == 8) {
        *(u32x4 *)(sm + hoff1 + 0 * 1024) = u32x4{hp1[0], hp1[1], hp1[2], hp1[3]};
        *(u32x4 *)(sm + hoff1 + 1 * 1024) = u32x4{hp1[4], hp1[5], hp1[6], hp1[7]};
      }
      SCHED0();
    }
    STAMP(22);
    __syncthreads();
    f32x16 o01, o11;
#pragma unroll
    for (int i = 0; i < 16; ++i) { o01[i] = 0.f; o11[i] = 0.f; }
#pragma unroll
    for (int s = 0; s < 3; ++s) bw[s] = *(const u32x4 *)(sm + G_H + ((1 * 14 + s) * 64 + lane) * 16);
    SCHED0();
    auto slab_store = [&](const f32x16 &a, int mt, int s2, int tl) {
      const u32x4 pk = {pack_bf16(a[8 * s2], a[8 * s2 + 1]), pack_bf16(a[8 * s2 + 2], a[8 * s2 + 3]), pack_bf16(a[8 * s2 + 4], a[8 * s2 + 5]), pack_bf16(a[8 * s2 + 6], a[8 * s2 + 7])};
      __builtin_amdgcn_raw_buffer_store_b128(pk, ws, slab, (4 * mt + 2 * s2) * 32768 + tl * 512, AUX_SC1);
    };
#pragma unroll
    for (int s = 0; s < 14; ++s) {
      const u32x4 cur = bw[s % 3];
      if (s + 3 < 14) bw[s % 3] = *(const u32x4 *)(sm + G_H + ((1 * 14 + s + 3) * 64 + lane) * 16);
      o01 = mfma_bf16(A4[s], cur, o01);
      o11 = mfma_bf16(A4[14 + s], cur, o11);
      if (s >= 1 && s < 5) slab_store((s - 1) >> 1 ? o10 : o00, (s - 1) >> 1, (s - 1) & 1, 0);
      SCHED0();
    }
    slab_store(o01, 0, 0, 1); slab_store(o01, 0, 1, 1); slab_store(o11, 1, 0, 1); slab_store(o11, 1, 1, 1);
  }
  const int fh = tid & 1, fks = (tid >> 1) & 3, ftk = tid >> 3;
  const int ftok = 64 * T + ftk, fkstep = 4 * j + fks, fchunk = 2 * fkstep + fh, ff0 = 16 * fkstep + 4 * fh;
  STAMP(23);
  DRAIN();
  __syncthreads();
  if (tid == 0) st32s(ws, WS_F3 + (unsigned)(T * 64 + j) * 4u, tag);
  float fbase[8];
  {
    const unsigned o = WS_HP + (unsigned)((ff0 / 2) * 2048 + ftok) * 4u;
    unsigned fhv[4];
    fhv[0] = __builtin_amdgcn_raw_buffer_load_b32(ws, o, 0, AUX_SC1);
    fhv[1] = __builtin_amdgcn_raw_buffer_load_b32(ws, o, 8192, AUX_SC1);
    fhv[2] = __builtin_amdgcn_raw_buffer_load_b32(ws, o, 4 * 8192, AUX_SC1);
    fhv[3] = __builtin_amdgcn_raw_buffer_load_b32(ws, o, 5 * 8192, AUX_SC1);
    const u32x4 fx1v = ld128s(ws, WS_X1 + (unsigned)(fchunk * 2048 + ftok) * 16u);
#pragma unroll
    for (int i = 0; i < 4; ++i) { fbase[2 * i] = bflo(fhv[i]) + bflo(fx1v[i]); fbase[2 * i + 1] = bfhi(fhv[i]) + bfhi(fx1v[i]); }
  }
  SCHED0();
  STAMP(24);
  if (w == 7) wait_flags8(ws, WS_F3 + (unsigned)T * 256u, tag, lane, 0, 8);
  __syncthreads();
  STAMP(25);
  {
    u32x4 sl[8];
    const unsigned o = WS_SLAB + (unsigned)(fchunk * 2048 + ftok) * 16u;
#pragma unroll
    for (int qq = 0; qq < 8; ++qq) sl[qq] = __builtin_amdgcn_raw_buffer_load_b128(ws, o, qq * 2097152, AUX_SC1);
#pragma unroll
    for (int qq = 0; qq < 8; ++qq)
#pragma unroll
      for (int i = 0; i < 4; ++i) { fbase[2 * i] += bflo(sl[qq][i]); fbase[2 * i + 1] += bfhi(sl[qq][i]); }
    GAS u16 *dst = (GAS u16 *)p.out + (size_t)ftok * D_ + ff0;
    *(GAS u32x2 *)dst = u32x2{pack_bf16(fbase[0], fbase[1]), pack_bf16(fbase[2], fbase[3])};
    *(GAS u32x2 *)(dst + 8) = u32x2{pack_bf16(fbase[4], fbase[5]), pack_bf16(fbase[6], fbase[7])};
  }
  if (b == 0 && tid == 0) st32s(ws, WS_EPOCH, tag);
  STAMP(26);
  asm volatile("k_all_end_marker:");
}

extern "C" void kernel_launch(void *const *d_in, const int *in_sizes, int n_in, void *d_out, int out_size, void *d_ws,
                              size_t ws_size, hipStream_t stream) {
  Params p;
  p.x = (const u16 *)d_in[0];
  p.logA = (const u16 *)d_in[1];
  p.Aim = (const u16 *)d_in[2];
  p.Cre = (const u16 *)d_in[3];
  p.Cim = (const u16 *)d_in[4];
  p.logdt = (const u16 *)d_in[5];
  p.Dskip = (const u16 *)d_in[6];
  p.w1 = (const u16 *)d_in[8];
  p.w2 = (const u16 *)d_in[9];
  p.w3 = (const u16 *)d_in[10];
  p.w4 = (const u16 *)d_in[11];
  p.out = (u16 *)d_out;
  p.ws = (unsigned char *)d_ws;
  k_all<<<256, 512, 0, stream>>>(p);
}
```
